```python
import torch

def _softmax(x, axis=-1):
    return torch.softmax(x, dim=axis)
def _gelu(x):
    return torch.nn.functional.gelu(x, approximate="tanh")
def _silu(x):
    return torch.nn.functional.silu(x)
def _sigmoid(x):
    return torch.sigmoid(x)
def _layernorm(x, axis=-1, eps=1e-5):
    m = x.mean(dim=axis, keepdim=True)
    v = x.var(dim=axis, keepdim=True, unbiased=False)
    return (x - m) / torch.sqrt(v + eps)
def _scatter_add(tgt, idx, src, axis=0):
    return tgt.clone().index_add_(axis, idx, src)
def _segment_sum(src, seg, n):
    out = torch.zeros((n,) + src.shape[1:], dtype=src.dtype)
    return out.index_add_(0, seg, src)
def _pad(x, pw):
    flat = tuple(v for pair in reversed(pw) for v in pair)
    return torch.nn.functional.pad(x, flat)

kernel_name = 'standalone__swiglu_ffn__sampling'

def setup_inputs():

    _w = (lambda a: (lambda t: t.to(torch.bfloat16) if t.is_floating_point() else t)(torch.as_tensor(a)))
    B, S, D, F = 16, 16, 1024, 3584
    return {
        "x": _w((lambda *s: torch.randn(*s, dtype=torch.bfloat16) * 0.05)(B, S, D)),
        "w_gate": _w((lambda *s: torch.randn(*s, dtype=torch.bfloat16) * 0.05)(D, F)),
        "w_up": _w((lambda *s: torch.randn(*s, dtype=torch.bfloat16) * 0.05)(D, F)),
        "w_down": _w((lambda *s: torch.randn(*s, dtype=torch.bfloat16) * 0.05)(F, D)),
    }

def reference(x, w_gate, w_up, w_down):
    g = _silu(x @ w_gate)
    u = x @ w_up
    return (g * u) @ w_down
```

```cpp
#include <hip/hip_runtime.h>
#include <hip/hip_bf16.h>
#include <stdint.h>

typedef unsigned v4u __attribute__((ext_vector_type(4)));
typedef float f32x4 __attribute__((ext_vector_type(4)));
typedef short bf16x8 __attribute__((ext_vector_type(8)));
#define GAS __attribute__((address_space(1)))
typedef GAS unsigned gu32;

constexpr int MM = 256, DD = 1024, FF = 3584;
constexpr int KSTEPS_F = FF / 32;

__device__ __forceinline__ f32x4 mfma(v4u a, v4u b, f32x4 c) {
  return __builtin_amdgcn_mfma_f32_16x16x32_bf16(__builtin_bit_cast(bf16x8, a), __builtin_bit_cast(bf16x8, b), c, 0, 0, 0);
}
__device__ __forceinline__ float bf16r(float f) {
  return (float)(__bf16)f;
}
__device__ __forceinline__ unsigned pack2(float lo, float hi) {
  __bf16 a = (__bf16)lo, b = (__bf16)hi;
  return (unsigned)__builtin_bit_cast(unsigned short, a) | ((unsigned)__builtin_bit_cast(unsigned short, b) << 16);
}
__device__ __forceinline__ v4u tfrag(const v4u (&w)[8], int t) {
  const unsigned sel = (t & 1) ? 0x07060302u : 0x05040100u;
  v4u r;
  r[0] = __builtin_amdgcn_perm(w[1][t >> 1], w[0][t >> 1], sel);
  r[1] = __builtin_amdgcn_perm(w[3][t >> 1], w[2][t >> 1], sel);
  r[2] = __builtin_amdgcn_perm(w[5][t >> 1], w[4][t >> 1], sel);
  r[3] = __builtin_amdgcn_perm(w[7][t >> 1], w[6][t >> 1], sel);
  return r;
}

constexpr int K1_WIN = 4;
__global__ __launch_bounds__(256, 1) void k_gateup(const uint16_t* __restrict__ x, const uint16_t* __restrict__ wg,
                                                   const uint16_t* __restrict__ wu, v4u* __restrict__ hfrag,
                                                   unsigned* __restrict__ cnt) {
  __shared__ f32x4 red[4 * 4 * 8 * 64];
  const int b = blockIdx.x, xcd = b & 7, i = b >> 3;
  const int ft = xcd * 7 + (i >> 2), mt4 = i & 3;
  const int lane = threadIdx.x & 63, w = __builtin_amdgcn_readfirstlane(threadIdx.x >> 6);
  const int g = lane >> 4, c = lane & 15;
  if (b == 0 && threadIdx.x < 64) cnt[threadIdx.x * 32] = 0u;

  const uint16_t* wbase = (c < 8 ? wg : wu) + (size_t)(w * 256 + 8 * g) * FF + ft * 64 + 8 * (c & 7);
  const uint16_t* xbase = x + (size_t)(mt4 * 64 + c) * DD + w * 256 + 8 * g;

  v4u wb[K1_WIN][8];
  v4u xb[K1_WIN][4];
#pragma unroll
  for (int s = 0; s < K1_WIN; ++s) {
#pragma unroll
    for (int mt = 0; mt < 4; ++mt) xb[s][mt] = *(const v4u*)(xbase + mt * 16 * DD + s * 32);
#pragma unroll
    for (int j = 0; j < 8; ++j) wb[s][j] = *(const v4u*)(wbase + (size_t)(s * 32 + j) * FF);
  }
  __builtin_amdgcn_sched_barrier(0);
  f32x4 acc[4][8];
#pragma unroll
  for (int mt = 0; mt < 4; ++mt)
#pragma unroll
    for (int t = 0; t < 8; ++t) acc[mt][t] = f32x4{0.f, 0.f, 0.f, 0.f};

#pragma unroll
  for (int s = 0; s < 8; ++s) {
    const int sl = s % K1_WIN;
    v4u bf[8], xa[4];
#pragma unroll
    for (int t = 0; t < 8; ++t) bf[t] = tfrag(wb[sl], t);
#pragma unroll
    for (int mt = 0; mt < 4; ++mt) xa[mt] = xb[sl][mt];
    __builtin_amdgcn_sched_barrier(0);
    if (s + K1_WIN < 8) {
#pragma unroll
      for (int mt = 0; mt < 4; ++mt) xb[sl][mt] = *(const v4u*)(xbase + mt * 16 * DD + (s + K1_WIN) * 32);
#pragma unroll
      for (int j = 0; j < 8; ++j) wb[sl][j] = *(const v4u*)(wbase + (size_t)((s + K1_WIN) * 32 + j) * FF);
    }
    __builtin_amdgcn_sched_barrier(0);
#pragma unroll
    for (int t = 0; t < 8; ++t)
#pragma unroll
      for (int mt = 0; mt < 4; ++mt) acc[mt][t] = mfma(xa[mt], bf[t], acc[mt][t]);
    __builtin_amdgcn_sched_barrier(0);
  }
#pragma unroll
  for (int mt = 0; mt < 4; ++mt)
    if (mt != w) {
#pragma unroll
      for (int t = 0; t < 8; ++t) red[((w * 4 + mt) * 8 + t) * 64 + lane] = acc[mt][t];
    }
  __syncthreads();
  f32x4 fin[8];
#pragma unroll
  for (int t = 0; t < 8; ++t) {
    f32x4 v = (w == 0) ? acc[0][t] : (w == 1) ? acc[1][t] : (w == 2) ? acc[2][t] : acc[3][t];
    fin[t] = v;
  }
#pragma unroll
  for (int ww = 0; ww < 4; ++ww)
    if (ww != w) {
#pragma unroll
      for (int t = 0; t < 8; ++t) fin[t] += red[((ww * 4 + w) * 8 + t) * 64 + lane];
    }
  const bool lo = c < 8;
  float hv[2][8];
#pragma unroll
  for (int t = 0; t < 8; ++t) {
#pragma unroll
    for (int r = 0; r < 2; ++r) {
      const float send = lo ? fin[t][2 + r] : fin[t][r];
      const float keep = lo ? fin[t][r] : fin[t][2 + r];
      const float recv = __builtin_bit_cast(float, __builtin_amdgcn_update_dpp(0, __builtin_bit_cast(int, send), 0x128  , 0xf, 0xf, false));
      const float gg = bf16r(lo ? keep : recv), uu = bf16r(lo ? recv : keep);
      const float sg = bf16r(gg / (1.0f + __expf(-gg)));
      hv[r][t] = sg * uu;
    }
  }
#pragma unroll
  for (int r = 0; r < 2; ++r) {
    v4u o;
    o[0] = pack2(hv[r][0], hv[r][1]);
    o[1] = pack2(hv[r][2], hv[r][3]);
    o[2] = pack2(hv[r][4], hv[r][5]);
    o[3] = pack2(hv[r][6], hv[r][7]);
    const int row = 4 * g + (lo ? r : 2 + r);
    const int ks = ft * 2 + ((c & 7) >> 2);
    hfrag[((size_t)(mt4 * 4 + w) * KSTEPS_F + ks) * 64 + (c & 3) * 16 + row] = o;
  }
}

__global__ __launch_bounds__(256, 1) void k_down(const v4u* __restrict__ hfrag, const uint16_t* __restrict__ wd,
                                                 uint16_t* __restrict__ out, f32x4* __restrict__ slab,
                                                 unsigned* __restrict__ cnt) {
  __shared__ f32x4 red[4 * 4 * 8 * 64];
  const int b = blockIdx.x, ks = b & 7, i = b >> 3;
  const int nt = i >> 2, mt4 = i & 3;
  const int lane = threadIdx.x & 63, w = __builtin_amdgcn_readfirstlane(threadIdx.x >> 6);
  const int g = lane >> 4, c = lane & 15;
  const int s0 = ks * 14 + (w < 2 ? w * 4 : 8 + (w - 2) * 3);
  const int ns = w < 2 ? 4 : 3;
  const uint16_t* wbase = wd + (size_t)(s0 * 32 + 8 * g) * DD + nt * 128 + 8 * c;
  const v4u* hbase = hfrag + ((size_t)(mt4 * 4) * KSTEPS_F + s0) * 64 + lane;

  v4u wb[4][8];
  v4u xb[4][4];
#pragma unroll
  for (int s = 0; s < 4; ++s) {
    if (s < ns) {
#pragma unroll
      for (int j = 0; j < 8; ++j) wb[s][j] = *(const v4u*)(wbase + (size_t)(s * 32 + j) * DD);
#pragma unroll
      for (int mt = 0; mt < 4; ++mt) xb[s][mt] = hbase[(size_t)(mt * KSTEPS_F + s) * 64];
    } else {
#pragma unroll
      for (int j = 0; j < 8; ++j) wb[s][j] = v4u{0, 0, 0, 0};
#pragma unroll
      for (int mt = 0; mt < 4; ++mt) xb[s][mt] = v4u{0, 0, 0, 0};
    }
  }
  __builtin_amdgcn_sched_barrier(0);
  f32x4 acc[4][8];
#pragma unroll
  for (int mt = 0; mt < 4; ++mt)
#pragma unroll
    for (int t = 0; t < 8; ++t) acc[mt][t] = f32x4{0.f, 0.f, 0.f, 0.f};
#pragma unroll
  for (int s = 0; s < 4; ++s) {
    if (s < ns) {
      v4u bf[8];
#pragma unroll
      for (int t = 0; t < 8; ++t) bf[t] = tfrag(wb[s], t);
#pragma unroll
      for (int t = 0; t < 8; ++t)
#pragma unroll
        for (int mt = 0; mt < 4; ++mt) acc[mt][t] = mfma(xb[s][mt], bf[t], acc[mt][t]);
    }
    __builtin_amdgcn_sched_barrier(0);
  }
#pragma unroll
  for (int mt = 0; mt < 4; ++mt)
    if (mt != w) {
#pragma unroll
      for (int t = 0; t < 8; ++t) red[((w * 4 + mt) * 8 + t) * 64 + lane] = acc[mt][t];
    }
  __syncthreads();
  f32x4 fin[8];
#pragma unroll
  for (int t = 0; t < 8; ++t) fin[t] = (w == 0) ? acc[0][t] : (w == 1) ? acc[1][t] : (w == 2) ? acc[2][t] : acc[3][t];
#pragma unroll
  for (int ww = 0; ww < 4; ++ww)
    if (ww != w) {
#pragma unroll
      for (int t = 0; t < 8; ++t) fin[t] += red[((ww * 4 + w) * 8 + t) * 64 + lane];
    }
  const int tile = i;
  const __amdgpu_buffer_rsrc_t rs = __builtin_amdgcn_make_buffer_rsrc((void*)slab, 0, 32 * 8 * 32768, 0x00020000);
#pragma unroll
  for (int t = 0; t < 8; ++t)
    __builtin_amdgcn_raw_buffer_store_b128(__builtin_bit_cast(v4u, fin[t]), rs, ((((tile * 8 + ks) * 4 + w) * 8 + t) * 64 + lane) * 16, 0, 16);
  asm volatile("s_waitcnt vmcnt(0)" ::: "memory");
  __syncthreads();
  if (w == 0) {
    gu32* cw = (gu32*)cnt + tile * 32;
    if (lane == 0) __hip_atomic_fetch_add(cw, 1u, __ATOMIC_RELAXED, __HIP_MEMORY_SCOPE_AGENT);
    while (__hip_atomic_load(cw, __ATOMIC_RELAXED, __HIP_MEMORY_SCOPE_AGENT) < 8u) __builtin_amdgcn_s_sleep(1);
    __builtin_amdgcn_fence(__ATOMIC_ACQUIRE, "wavefront");
    if (lane < 32) {
      const int rg = 2 * ks + (lane >> 4);
      const int w2 = rg >> 2, g2 = rg & 3;
      const int l2 = g2 * 16 + c;
      f32x4 sum[8];
#pragma unroll
      for (int t = 0; t < 8; ++t) sum[t] = f32x4{0.f, 0.f, 0.f, 0.f};
      v4u ld[8][8];
#pragma unroll
      for (int q = 0; q < 8; ++q)
#pragma unroll
        for (int t = 0; t < 8; ++t)
          ld[q][t] = __builtin_amdgcn_raw_buffer_load_b128(rs, ((((tile * 8 + q) * 4 + w2) * 8 + t) * 64 + l2) * 16, 0, 16);
#pragma unroll
      for (int q = 0; q < 8; ++q)
#pragma unroll
        for (int t = 0; t < 8; ++t) sum[t] += __builtin_bit_cast(f32x4, ld[q][t]);
#pragma unroll
      for (int r = 0; r < 4; ++r) {
        v4u o;
        o[0] = pack2(sum[0][r], sum[1][r]);
        o[1] = pack2(sum[2][r], sum[3][r]);
        o[2] = pack2(sum[4][r], sum[5][r]);
        o[3] = pack2(sum[6][r], sum[7][r]);
        const int row = mt4 * 64 + w2 * 16 + g2 * 4 + r;
        *(v4u*)(out + (size_t)row * DD + nt * 128 + 8 * c) = o;
      }
    }
  }
}

extern "C" void kernel_launch(void* const* d_in, const int* in_sizes, int n_in, void* d_out, int out_size, void* d_ws,
                              size_t ws_size, hipStream_t stream) {
  const uint16_t* x = (const uint16_t*)d_in[0];
  const uint16_t* wg = (const uint16_t*)d_in[1];
  const uint16_t* wu = (const uint16_t*)d_in[2];
  const uint16_t* wd = (const uint16_t*)d_in[3];
  unsigned char* ws = (unsigned char*)d_ws;
  unsigned* cnt = (unsigned*)ws;
  v4u* hfrag = (v4u*)(ws + 65536);
  f32x4* slab = (f32x4*)(ws + 65536 + (2u << 20));
  k_gateup<<<224, 256, 0, stream>>>(x, wg, wu, hfrag, cnt);
  k_down<<<256, 256, 0, stream>>>(hfrag, wd, (uint16_t*)d_out, slab, cnt);
}
```

```cpp
#include <hip/hip_runtime.h>
#include <hip/hip_bf16.h>
#include <stdint.h>

typedef unsigned v4u __attribute__((ext_vector_type(4)));
typedef float f32x4 __attribute__((ext_vector_type(4)));
typedef short bf16x8 __attribute__((ext_vector_type(8)));
#define GAS __attribute__((address_space(1)))
typedef GAS unsigned gu32;

#define STAMP(kid, k)
#define STAMP_DECL
#define STAMP_END(kid)
#define NT_W 1
#define K1_WIN 3
#define K2_WIN 2

constexpr int MM = 256, DD = 1024, FF = 3584;
constexpr int KSTEPS_F = FF / 32;

__device__ __forceinline__ f32x4 mfma(v4u a, v4u b, f32x4 c) {
  return __builtin_amdgcn_mfma_f32_16x16x32_bf16(__builtin_bit_cast(bf16x8, a), __builtin_bit_cast(bf16x8, b), c, 0, 0, 0);
}
__device__ __forceinline__ float bf16r(float f) { return (float)(__bf16)f; }
__device__ __forceinline__ unsigned pack2(float lo, float hi) {
  __bf16 a = (__bf16)lo, b = (__bf16)hi;
  return (unsigned)__builtin_bit_cast(unsigned short, a) | ((unsigned)__builtin_bit_cast(unsigned short, b) << 16);
}
__device__ __forceinline__ v4u ldw(const uint16_t* p) {
  return __builtin_nontemporal_load((const v4u*)p);
}
__device__ __forceinline__ v4u tfrag(const v4u (&w)[8], int t) {
  const unsigned sel = (t & 1) ? 0x07060302u : 0x05040100u;
  v4u r;
  r[0] = __builtin_amdgcn_perm(w[1][t >> 1], w[0][t >> 1], sel);
  r[1] = __builtin_amdgcn_perm(w[3][t >> 1], w[2][t >> 1], sel);
  r[2] = __builtin_amdgcn_perm(w[5][t >> 1], w[4][t >> 1], sel);
  r[3] = __builtin_amdgcn_perm(w[7][t >> 1], w[6][t >> 1], sel);
  return r;
}

__global__ __launch_bounds__(256, 1) void k_gateup(const uint16_t* __restrict__ x, const uint16_t* __restrict__ wg,
                                                   const uint16_t* __restrict__ wu, v4u* __restrict__ hfrag,
                                                   unsigned* __restrict__ cnt) {
  __shared__ f32x4 red[4 * 4 * 8 * 64];
  STAMP_DECL
  STAMP(0, 0);
  const int b = blockIdx.x, xcd = b & 7, i = b >> 3;
  const int ft = xcd * 7 + (i >> 2), mt4 = i & 3;
  const int lane = threadIdx.x & 63, w = __builtin_amdgcn_readfirstlane(threadIdx.x >> 6);
  const int g = lane >> 4, c = lane & 15;
  if (b == 0 && threadIdx.x < 64) cnt[threadIdx.x * 32] = 0u;

  const uint16_t* wbase = (c < 8 ? wg : wu) + (size_t)(w * 256 + 8 * g) * FF + ft * 64 + 8 * (c & 7);
  v4u* xl = (v4u*)red + w * 2048;
  const uint16_t* xsrc = x + (size_t)(mt4 * 64 + (lane >> 3)) * DD + w * 256 + (lane & 7) * 8;
  v4u xv[2][8];
  v4u wb[K1_WIN][8];
#define XLOAD(p) _Pragma("unroll") for (int r = 0; r < 8; ++r) xv[(p) & 1][r] = *(const v4u*)(xsrc + (size_t)8 * r * DD + (p) * 64);
#define XSTORE(p) _Pragma("unroll") for (int r = 0; r < 8; ++r) { const int row = 8 * r + (lane >> 3); xl[row * 32 + (((p) * 8 + (lane & 7)) ^ (row & 15))] = xv[(p) & 1][r]; }
#define WLOAD(s) _Pragma("unroll") for (int j = 0; j < 8; ++j) wb[(s) % K1_WIN][j] = ldw(wbase + (size_t)((s) * 32 + j) * FF);
  XLOAD(0)
  WLOAD(0)
  WLOAD(1)
  XLOAD(1)
#pragma unroll
  for (int s = 2; s < K1_WIN; ++s) { WLOAD(s) }
  __builtin_amdgcn_sched_barrier(0);
  f32x4 acc[4][8];
#pragma unroll
  for (int mt = 0; mt < 4; ++mt)
#pragma unroll
    for (int t = 0; t < 8; ++t) acc[mt][t] = f32x4{0.f, 0.f, 0.f, 0.f};

#pragma unroll
  for (int s = 0; s < 8; ++s) {
    if ((s & 1) == 0) {
      XSTORE(s >> 1)
      __builtin_amdgcn_sched_barrier(0);
      if ((s >> 1) + 2 < 4) { XLOAD((s >> 1) + 2) }
      __builtin_amdgcn_sched_barrier(0);
    }
    v4u bf[8], xa[4];
#pragma unroll
    for (int r = 0; r < 4; ++r) xa[r] = xl[((((w + r) & 3) * 16 + c) * 32) + ((s * 4 + g) ^ c)];
#pragma unroll
    for (int t = 0; t < 8; ++t) bf[t] = tfrag(wb[s % K1_WIN], t);
    __builtin_amdgcn_sched_barrier(0);
    if (s + K1_WIN < 8) { WLOAD(s + K1_WIN) }
    __builtin_amdgcn_sched_barrier(0);
    if (s == 0) STAMP(0, 1);
#pragma unroll
    for (int t = 0; t < 8; ++t)
#pragma unroll
      for (int r = 0; r < 4; ++r) acc[r][t] = mfma(xa[r], bf[t], acc[r][t]);
    __builtin_amdgcn_sched_barrier(0);
    if (s == 3) STAMP(0, 2);
  }
  STAMP(0, 3);
  __syncthreads();
#pragma unroll
  for (int r = 1; r < 4; ++r)
#pragma unroll
    for (int t = 0; t < 8; ++t) red[(((w + r) & 3) * 4 + r) * 512 + t * 64 + lane] = acc[r][t];
  __syncthreads();
#pragma unroll
  for (int r = 1; r < 4; ++r)
#pragma unroll
    for (int t = 0; t < 8; ++t) acc[0][t] += red[(w * 4 + r) * 512 + t * 64 + lane];
  STAMP(0, 4);
  const bool lo = c < 8;
  float hv[2][8];
#pragma unroll
  for (int t = 0; t < 8; ++t) {
#pragma unroll
    for (int r = 0; r < 2; ++r) {
      const float send = lo ? acc[0][t][2 + r] : acc[0][t][r];
      const float keep = lo ? acc[0][t][r] : acc[0][t][2 + r];
      const float recv = __builtin_bit_cast(float, __builtin_amdgcn_update_dpp(0, __builtin_bit_cast(int, send), 0x128  , 0xf, 0xf, false));
      const float gg = bf16r(lo ? keep : recv), uu = bf16r(lo ? recv : keep);
      const float sg = bf16r(gg * __builtin_amdgcn_rcpf(1.0f + __expf(-gg)));
      hv[r][t] = sg * uu;
    }
  }
#pragma unroll
  for (int r = 0; r < 2; ++r) {
    v4u o;
    o[0] = pack2(hv[r][0], hv[r][1]);
    o[1] = pack2(hv[r][2], hv[r][3]);
    o[2] = pack2(hv[r][4], hv[r][5]);
    o[3] = pack2(hv[r][6], hv[r][7]);
    const int row = 4 * g + (lo ? r : 2 + r);
    const int ks = ft * 2 + ((c & 7) >> 2);
    hfrag[((size_t)(mt4 * 4 + w) * KSTEPS_F + ks) * 64 + (c & 3) * 16 + row] = o;
  }
  STAMP(0, 5);
  STAMP_END(0);
}

__global__ __launch_bounds__(256, 1) void k_down(const v4u* __restrict__ hfrag, const uint16_t* __restrict__ wd,
                                                 uint16_t* __restrict__ out, f32x4* __restrict__ slab,
                                                 unsigned* __restrict__ cnt) {
  __shared__ f32x4 red[4 * 4 * 8 * 64];
  STAMP_DECL
  STAMP(1, 0);
  const int b = blockIdx.x, ks = b & 7, i = b >> 3;
  const int nt = i >> 2, mt4 = i & 3;
  const int lane = threadIdx.x & 63, w = __builtin_amdgcn_readfirstlane(threadIdx.x >> 6);
  const int g = lane >> 4, c = lane & 15;
  const int s0 = ks * 14 + (w < 2 ? w * 4 : 8 + (w - 2) * 3);
  const int ns = w < 2 ? 4 : 3;
  const uint16_t* wbase = wd + (size_t)(s0 * 32 + 8 * g) * DD + nt * 128 + 8 * c;
  const v4u* hbase = hfrag + ((size_t)(mt4 * 4) * KSTEPS_F + s0) * 64 + lane;

  v4u wb[K2_WIN][8];
  v4u xb[K2_WIN][4];
#define W2LOAD(s) { _Pragma("unroll") for (int j = 0; j < 8; ++j) wb[(s) % K2_WIN][j] = ldw(wbase + (size_t)((s) * 32 + j) * DD); \
                    _Pragma("unroll") for (int r = 0; r < 4; ++r) xb[(s) % K2_WIN][r] = hbase[(size_t)(((w + r) & 3) * KSTEPS_F + (s)) * 64]; }
#pragma unroll
  for (int s = 0; s < K2_WIN; ++s) W2LOAD(s)
  __builtin_amdgcn_sched_barrier(0);
  f32x4 acc[4][8];
#pragma unroll
  for (int mt = 0; mt < 4; ++mt)
#pragma unroll
    for (int t = 0; t < 8; ++t) acc[mt][t] = f32x4{0.f, 0.f, 0.f, 0.f};
#pragma unroll
  for (int s = 0; s < 4; ++s) {
    if (s < ns) {
      v4u bf[8], xa[4];
#pragma unroll
      for (int t = 0; t < 8; ++t) bf[t] = tfrag(wb[s % K2_WIN], t);
#pragma unroll
      for (int r = 0; r < 4; ++r) xa[r] = xb[s % K2_WIN][r];
      __builtin_amdgcn_sched_barrier(0);
      if (s + K2_WIN < 4 && s + K2_WIN < ns) W2LOAD(s + K2_WIN)
      __builtin_amdgcn_sched_barrier(0);
      if (s == 0) STAMP(1, 1);
#pragma unroll
      for (int t = 0; t < 8; ++t)
#pragma unroll
        for (int r = 0; r < 4; ++r) acc[r][t] = mfma(xa[r], bf[t], acc[r][t]);
    }
    __builtin_amdgcn_sched_barrier(0);
  }
  STAMP(1, 2);
#pragma unroll
  for (int r = 1; r < 4; ++r)
#pragma unroll
    for (int t = 0; t < 8; ++t) red[(((w + r) & 3) * 4 + r) * 512 + t * 64 + lane] = acc[r][t];
  __syncthreads();
#pragma unroll
  for (int r = 1; r < 4; ++r)
#pragma unroll
    for (int t = 0; t < 8; ++t) acc[0][t] += red[(w * 4 + r) * 512 + t * 64 + lane];
  STAMP(1, 3);
  const int tile = i;
  const __amdgpu_buffer_rsrc_t rs = __builtin_amdgcn_make_buffer_rsrc((void*)slab, 0, 32 * 8 * 32768, 0x00020000);
#pragma unroll
  for (int t = 0; t < 8; ++t)
    __builtin_amdgcn_raw_buffer_store_b128(__builtin_bit_cast(v4u, acc[0][t]), rs, ((((tile * 8 + ks) * 4 + w) * 8 + t) * 64 + lane) * 16, 0, 16);
  asm volatile("s_waitcnt vmcnt(0)" ::: "memory");
  __syncthreads();
  STAMP(1, 4);
  if (w == 0) {
    gu32* cw = (gu32*)cnt + tile * 32;
    if (lane == 0) __hip_atomic_fetch_add(cw, 1u, __ATOMIC_RELAXED, __HIP_MEMORY_SCOPE_AGENT);
    while (__hip_atomic_load(cw, __ATOMIC_RELAXED, __HIP_MEMORY_SCOPE_AGENT) < 8u) __builtin_amdgcn_s_sleep(1);
    __builtin_amdgcn_fence(__ATOMIC_ACQUIRE, "wavefront");
    STAMP(1, 5);
    if (lane < 32) {
      const int rg = 2 * ks + (lane >> 4);
      const int w2 = rg >> 2, g2 = rg & 3;
      const int l2 = g2 * 16 + c;
      f32x4 sum[8];
#pragma unroll
      for (int t = 0; t < 8; ++t) sum[t] = f32x4{0.f, 0.f, 0.f, 0.f};
      v4u ld[8][8];
#pragma unroll
      for (int q = 0; q < 8; ++q)
#pragma unroll
        for (int t = 0; t < 8; ++t)
          ld[q][t] = __builtin_amdgcn_raw_buffer_load_b128(rs, ((((tile * 8 + q) * 4 + w2) * 8 + t) * 64 + l2) * 16, 0, 16);
#pragma unroll
      for (int q = 0; q < 8; ++q)
#pragma unroll
        for (int t = 0; t < 8; ++t) sum[t] += __builtin_bit_cast(f32x4, ld[q][t]);
#pragma unroll
      for (int r = 0; r < 4; ++r) {
        v4u o;
        o[0] = pack2(sum[0][r], sum[1][r]);
        o[1] = pack2(sum[2][r], sum[3][r]);
        o[2] = pack2(sum[4][r], sum[5][r]);
        o[3] = pack2(sum[6][r], sum[7][r]);
        const int row = mt4 * 64 + w2 * 16 + g2 * 4 + r;
        *(v4u*)(out + (size_t)row * DD + nt * 128 + 8 * c) = o;
      }
    }
    STAMP(1, 6);
  }
  STAMP_END(1);
}

extern "C" void kernel_launch(void* const* d_in, const int* in_sizes, int n_in, void* d_out, int out_size, void* d_ws,
                              size_t ws_size, hipStream_t stream) {
  const uint16_t* x = (const uint16_t*)d_in[0];
  const uint16_t* wg = (const uint16_t*)d_in[1];
  const uint16_t* wu = (const uint16_t*)d_in[2];
  const uint16_t* wd = (const uint16_t*)d_in[3];
  unsigned char* ws = (unsigned char*)d_ws;
  unsigned* cnt = (unsigned*)ws;
  v4u* hfrag = (v4u*)(ws + 65536);
  f32x4* slab = (f32x4*)(ws + 65536 + (2u << 20));
  k_gateup<<<224, 256, 0, stream>>>(x, wg, wu, hfrag, cnt);
  k_down<<<256, 256, 0, stream>>>(hfrag, wd, (uint16_t*)d_out, slab, cnt);
}
```

```cpp
#define SPF 28
#include <hip/hip_runtime.h>
#include <hip/hip_bf16.h>
#include <stdint.h>

#define LAS __attribute__((address_space(3)))
constexpr int DD = 1024, FF = 3584;
#define LDSB 163840
#define ROTM 3

__global__ __launch_bounds__(256, 1) void k_ffn(const uint16_t* __restrict__ x, const uint16_t* __restrict__ wg,
                                                const uint16_t* __restrict__ wu, const uint16_t* __restrict__ wd,
                                                uint16_t* __restrict__ out, unsigned char* __restrict__ ws) {
  __shared__ __attribute__((aligned(16))) unsigned char lds[LDSB];
  const int b = ((((int)blockIdx.x >> 3) + SPF) & 31) * 8 + ((int)blockIdx.x & 7), xcd = b & 7, i = b >> 3;
  const int lane = threadIdx.x & 63, w = __builtin_amdgcn_readfirstlane(threadIdx.x >> 6);
  const int jj = i >> 2, mt4 = i & 3;
  const int ft = xcd * 7 + (jj < 7 ? jj : 6);
  const int rt = mt4 & ROTM;
  const int rho = lane >> 3, gam = lane & 7;
  const unsigned voa = (16 * (w & 1) + 8 * (rho >> 2) + (rho & 3)) * (FF * 2) + ((((gam >> 1) - (rho >> 1)) & 3) * 32 + (gam & 1) * 16);
  const uint16_t* wb = ((w >> 1) ? wu : wg) + ft * 64;
  const uint16_t* wq0 = wb + (size_t)((0 + rt) & 3) * 256 * FF;
  const uint16_t* wq1 = wb + (size_t)((1 + rt) & 3) * 256 * FF;
  const uint16_t* wq2 = wb + (size_t)((2 + rt) & 3) * 256 * FF;
  const uint16_t* wq3 = wb + (size_t)((3 + rt) & 3) * 256 * FF;
  const unsigned xo0 = rho * 2048 + (gam ^ rho) * 16;
  const unsigned xo1 = (lane & 15) * 2048 + (lane >> 4) * 16;
  const uint16_t* xb = x + (size_t)(mt4 * 64 + w * 16) * DD;
  const uint16_t* xq0 = xb + ((0 + rt) & 3) * 256;
  const uint16_t* xq1 = xb + ((1 + rt) & 3) * 256;
  const uint16_t* xq2 = xb + ((2 + rt) & 3) * 256;
  const uint16_t* xq3 = xb + ((3 + rt) & 3) * 256;
  const unsigned lb = (unsigned)(uintptr_t)(LAS unsigned char*)lds;
  const unsigned m0b = lb + w * 2048;
#define OT0 0xfedcba9876543210ull
#define OT1 0xfedcba9876543210ull
#define OT2 0xfedcba9876543210ull
#define OT3 0xfedcba9876543210ull
  const unsigned long long ot = mt4 == 0 ? OT0 : mt4 == 1 ? OT1 : mt4 == 2 ? OT2 : OT3;
  const unsigned ot0 = (unsigned)ot, ot1 = (unsigned)(ot >> 32);
  asm volatile(
      "s_mov_b32 s64, m0\n"
      "s_add_u32 m0, %[m0b], 155648\n"
      "v_mov_b32 v88, %[voa]\n"
      "v_mov_b32 v90, %[xo0]\n"
      "s_cmp_eq_u32 %[j], 7\n"
      "s_cbranch_scc1 .Lspare_%=\n"
      "v_add_u32 v89, 0x7000, %[voa]\n"
      "v_add_u32 v91, 0x4000, %[xo0]\n"
      "s_mov_b64 s[58:59], %[xq0]\n"
      "global_load_lds_dwordx4 v90, s[58:59] offset:0\n"
      "s_add_u32 m0, %[m0b], 156672\n"
      "s_nop 0\n"
      "global_load_lds_dwordx4 v91, s[58:59] offset:0\n"
      "s_add_u32 m0, %[m0b], 147456\n"
      "s_mov_b64 s[60:61], %[wq0]\n"
      "s_add_u32 s82, s60, 0x7000\n"
      "s_addc_u32 s83, s61, 0\n"
      "global_load_lds_dwordx4 v88, s[60:61] \n"
      "s_add_u32 m0, %[m0b], 148480\n"
      "s_nop 0\n"
      "global_load_lds_dwordx4 v88, s[82:83] \n"
      "s_add_u32 m0, %[m0b], 139264\n"
      "v_add_u32 v88, 0x38000, v88\n"
      "global_load_lds_dwordx4 v88, s[60:61] \n"
      "s_add_u32 m0, %[m0b], 140288\n"
      "s_nop 0\n"
      "global_load_lds_dwordx4 v88, s[82:83] \n"
      "s_add_u32 m0, %[m0b], 130944\n"
      "s_nop 0\n"
      "global_load_lds_dwordx4 v90, s[58:59] offset:128\n"
      "s_add_u32 m0, %[m0b], 131968\n"
      "s_nop 0\n"
      "global_load_lds_dwordx4 v91, s[58:59] offset:128\n"
      "s_add_u32 m0, %[m0b], 122880\n"
      "v_add_u32 v88, 0x38000, v88\n"
      "global_load_lds_dwordx4 v88, s[60:61] \n"
      "s_add_u32 m0, %[m0b], 123904\n"
      "s_nop 0\n"
      "global_load_lds_dwordx4 v88, s[82:83] \n"
      "s_add_u32 m0, %[m0b], 114688\n"
      "v_add_u32 v88, 0x38000, v88\n"
      "global_load_lds_dwordx4 v88, s[60:61] \n"
      "s_add_u32 m0, %[m0b], 115712\n"
      "s_nop 0\n"
      "global_load_lds_dwordx4 v88, s[82:83] \n"
      "s_add_u32 m0, %[m0b], 106240\n"
      "s_lshl_b32 s67, %[j], 2\n"
      "s_add_u32 s67, s67, %[w]\n"
      "s_mul_i32 s67, s67, 30\n"
      "v_min_u32 v24, 29, %[ln]\n"
      "v_add_u32 v24, s67, v24\n"
      "v_min_u32 v24, 831, v24\n"
      "v_mul_u32_u24 v25, 5042, v24\n"
      "v_lshrrev_b32 v25, 16, v25\n"
      "v_mul_u32_u24 v26, 13, v25\n"
      "v_sub_u32 v24, v24, v26\n"
      "s_lshl_b32 s67, %[mt4], 2\n"
      "s_add_u32 s84, s67, 3\n"
      "v_add_u32 v24, s84, v24\n"
      "v_and_b32 v24, 15, v24\n"
      "v_lshlrev_b32 v24, 7, v24\n"
      "v_lshl_add_u32 v24, v25, 11, v24\n"
      "s_lshl_b32 s67, s67, 7\n"
      "s_lshl_b32 s84, %[w], 15\n"
      "s_add_u32 s67, s67, s84\n"
      "s_mov_b64 s[84:85], %[xq0]\n"
      "s_sub_u32 s84, s84, s67\n"
      "s_subb_u32 s85, s85, 0\n"
      "global_load_dword v25, v24, s[84:85]\n"
      "global_load_lds_dwordx4 v90, s[58:59] offset:256\n"
      "s_add_u32 m0, %[m0b], 107264\n"
      "s_nop 0\n"
      "global_load_lds_dwordx4 v91, s[58:59] offset:256\n"
      "s_add_u32 m0, %[m0b], 98304\n"
      "v_add_u32 v88, 0x38000, v88\n"
      "global_load_lds_dwordx4 v88, s[60:61] \n"
      "s_add_u32 m0, %[m0b], 99328\n"
      "s_nop 0\n"
      "global_load_lds_dwordx4 v88, s[82:83] \n"
      "s_add_u32 m0, %[m0b], 90112\n"
      "s_mov_b32 s94, 0x5ac3e7b1\n"
      "s_mov_b32 s95, 0x9d2f\n"
      "s_mov_b32 s96, 0\n"
      "s_mov_b32 s62, 0xbfb8aa3b\n"
      "s_mov_b32 s63, 0xbfb8aa3b\n"
      "s_mov_b64 s[54:55], %[ws]\n"
      "v_lshlrev_b32 v4, 4, %[ln]\n"
      "s_mul_i32 s67, %[bb], 32768\n"
      "s_mul_i32 s86, %[w], 8192\n"
      "s_add_u32 s67, s67, s86\n"
      "s_add_u32 s67, s67, 2101248\n"
      "s_add_u32 s40, s54, s67\n"
      "s_addc_u32 s41, s55, 0\n"
      "s_lshl_b32 s85, %[mt4], 2\n"
      "s_add_u32 s85, s85, %[w]\n"
      "s_mul_i32 s67, s85, 112\n"
      "s_mul_i32 s84, %[xcd], 14\n"
      "s_add_u32 s67, s67, s84\n"
      "s_lshl_b32 s87, s67, 10\n"
      "s_add_u32 s50, s54, s87\n"
      "s_addc_u32 s51, s55, 0\n"
      "s_and_b32 s84, %[w], 1\n"
      "s_mul_i32 s84, s84, 113664\n"
      "s_sub_u32 s52, s50, s84\n"
      "s_subb_u32 s53, s51, 0\n"
      "s_lshl_b32 s67, %[j], 11\n"
      "s_add_u32 s42, s52, s67\n"
      "s_addc_u32 s43, s53, 0\n"
      "v_add_u32 v105, 0x1c000, v4\n"
      "s_lshl_b32 s84, %[w], 10\n"
      "s_and_b32 s67, %[w], 2\n"
      "s_lshl_b32 s67, s67, 10\n"
      "s_add_u32 s84, s84, s67\n"
      "s_add_u32 s84, s84, %[lb]\n"
      "s_add_u32 s84, s84, 0\n"
      "v_add_u32 v106, s84, v4\n"
      "s_add_u32 s84, %[m0b], 0\n"
      "v_add_u32 v107, s84, v4\n"
      "v_bfe_u32 v6, %[ln], 2, 3\n"
      "v_lshrrev_b32 v8, 5, %[ln]\n"
      "v_lshlrev_b32 v8, 11, v8\n"
      "v_lshl_add_u32 v8, v6, 7, v8\n"
      "v_and_b32 v10, 3, %[ln]\n"
      "v_lshl_add_u32 v8, v10, 3, v8\n"
      "v_add_u32 v8, %[lb], v8\n"
      "v_lshrrev_b32 v10, 1, v6\n"
      "v_add_u32 v11, 0, v10\n"
      "v_and_b32 v11, 3, v11\n"
      "v_lshl_add_u32 v12, v11, 5, v8\n"
      "v_add_u32 v16, 0x10000, v12\n"
      "v_add_u32 v20, 0x20000, v12\n"
      "v_add_u32 v11, 1, v10\n"
      "v_and_b32 v11, 3, v11\n"
      "v_lshl_add_u32 v13, v11, 5, v8\n"
      "v_add_u32 v17, 0x10000, v13\n"
      "v_add_u32 v21, 0x20000, v13\n"
      "v_add_u32 v11, 2, v10\n"
      "v_and_b32 v11, 3, v11\n"
      "v_lshl_add_u32 v14, v11, 5, v8\n"
      "v_add_u32 v18, 0x10000, v14\n"
      "v_add_u32 v22, 0x20000, v14\n"
      "v_add_u32 v11, 3, v10\n"
      "v_and_b32 v11, 3, v11\n"
      "v_lshl_add_u32 v15, v11, 5, v8\n"
      "v_add_u32 v19, 0x10000, v15\n"
      "v_add_u32 v23, 0x20000, v15\n"
      "s_lshl_b32 s84, %[w], 1\n"
      "v_add_u32 v11, s84, v10\n"
      "v_and_b32 v11, 3, v11\n"
      "v_lshl_add_u32 v98, v11, 5, v8\n"
      "v_add_u32 v100, 0x10000, v98\n"
      "v_add_u32 v102, 0x20000, v98\n"
      "v_add_u32 v11, s84, v10\n"
      "v_add_u32 v11, 1, v11\n"
      "v_and_b32 v11, 3, v11\n"
      "v_lshl_add_u32 v99, v11, 5, v8\n"
      "v_add_u32 v101, 0x10000, v99\n"
      "v_add_u32 v103, 0x20000, v99\n"
      "v_and_b32 v6, 7, %[ln]\n"
      "v_lshrrev_b32 v8, 4, %[ln]\n"
      "v_xor_b32 v8, v8, v6\n"
      "v_lshlrev_b32 v8, 4, v8\n"
      "v_lshl_add_u32 v8, v6, 7, v8\n"
      "v_bfe_u32 v10, %[ln], 3, 1\n"
      "v_lshl_add_u32 v8, v10, 10, v8\n"
      "s_and_b32 s84, %[w], 2\n"
      "s_lshl_b32 s84, s84, 11\n"
      "s_add_u32 s84, s84, %[lb]\n"
      "v_add_u32 v92, s84, v8\n"
      "v_xor_b32 v93, 64, v92\n"
      "v_add_u32 v94, 0x10000, v92\n"
      "v_add_u32 v95, 0x10000, v93\n"
      "v_add_u32 v96, 0x20000, v92\n"
      "v_add_u32 v97, 0x20000, v93\n"
      "v_lshrrev_b32 v6, 3, %[ln]\n"
      "v_bfe_u32 v8, %[ln], 1, 2\n"
      "v_lshrrev_b32 v10, 1, v6\n"
      "v_sub_u32 v8, v8, v10\n"
      "v_xor_b32 v8, %[xcd], v8\n"
      "v_and_b32 v8, 3, v8\n"
      "v_and_b32 v10, 1, %[ln]\n"
      "v_lshlrev_b32 v10, 4, v10\n"
      "v_lshl_add_u32 v8, v8, 5, v10\n"
      "v_lshl_add_u32 v8, v6, 11, v8\n"
      "s_and_b32 s67, %[w], 1\n"
      "s_lshl_b32 s67, s67, 14\n"
      "s_lshr_b32 s84, %[w], 1\n"
      "s_lshr_b32 s57, %[xcd], 2\n"
      "s_xor_b32 s84, s84, s57\n"
      "s_lshl_b32 s84, s84, 7\n"
      "s_add_u32 s67, s67, s84\n"
      "v_add_u32 v9, s67, v8\n"
      "s_mul_i32 s67, %[xcd], 0xe0000\n"
      "s_lshl_b32 s84, %[j], 8\n"
      "s_add_u32 s67, s67, s84\n"
      "s_mov_b64 s[44:45], %[wd]\n"
      "s_add_u32 s44, s44, s67\n"
      "s_addc_u32 s45, s45, 0\n"
      "s_lshl_b32 s56, %[j], 17\n"
      "s_cmp_eq_u32 s56, 0xe0000\n"
      "s_cselect_b32 s56, 0, s56\n"
      "s_lshr_b32 s57, s56, 6\n"
      "s_add_u32 s68, s50, s57\n"
      "s_addc_u32 s69, s51, 0\n"
      "s_add_u32 s57, s57, 0x800\n"
      "s_cmp_eq_u32 s57, 0x3800\n"
      "s_cselect_b32 s57, 0, s57\n"
      "s_add_u32 s70, s50, s57\n"
      "s_addc_u32 s71, s51, 0\n"
      "s_add_u32 s57, s57, 0x800\n"
      "s_cmp_eq_u32 s57, 0x3800\n"
      "s_cselect_b32 s57, 0, s57\n"
      "s_add_u32 s72, s50, s57\n"
      "s_addc_u32 s73, s51, 0\n"
      "s_add_u32 s57, s57, 0x800\n"
      "s_cmp_eq_u32 s57, 0x3800\n"
      "s_cselect_b32 s57, 0, s57\n"
      "s_add_u32 s74, s50, s57\n"
      "s_addc_u32 s75, s51, 0\n"
      "s_add_u32 s57, s57, 0x800\n"
      "s_cmp_eq_u32 s57, 0x3800\n"
      "s_cselect_b32 s57, 0, s57\n"
      "s_add_u32 s76, s50, s57\n"
      "s_addc_u32 s77, s51, 0\n"
      "s_add_u32 s57, s57, 0x800\n"
      "s_cmp_eq_u32 s57, 0x3800\n"
      "s_cselect_b32 s57, 0, s57\n"
      "s_add_u32 s78, s50, s57\n"
      "s_addc_u32 s79, s51, 0\n"
      "s_add_u32 s57, s57, 0x800\n"
      "s_cmp_eq_u32 s57, 0x3800\n"
      "s_cselect_b32 s57, 0, s57\n"
      "s_add_u32 s80, s50, s57\n"
      "s_addc_u32 s81, s51, 0\n"
      "s_lshr_b32 s67, %[bb], 3\n"
      "s_mul_i32 s67, s67, 262144\n"
      "s_add_u32 s67, s67, s86\n"
      "s_add_u32 s67, s67, 2097152\n"
      "s_add_u32 s46, s54, s67\n"
      "s_addc_u32 s47, s55, 0\n"
      "s_lshl_b32 s67, s85, 15\n"
      "s_lshl_b32 s84, %[j], 8\n"
      "s_add_u32 s67, s67, s84\n"
      "s_lshl_b32 s84, %[xcd], 5\n"
      "s_add_u32 s67, s67, s84\n"
      "s_mov_b64 s[48:49], %[out]\n"
      "s_add_u32 s48, s48, s67\n"
      "s_addc_u32 s49, s49, 0\n"
      "s_lshl_b32 s67, %[bb], 9\n"
      "s_lshl_b32 s84, %[w], 7\n"
      "s_add_u32 s67, s67, s84\n"
      "s_add_u32 s67, s67, 0xc00000\n"
      "v_and_b32 v8, 15, %[ln]\n"
      "v_lshl_add_u32 v5, v8, 3, s67\n"
      "s_andn2_b32 s67, %[bb], 7\n"
      "s_lshl_b32 s67, s67, 9\n"
      "s_add_u32 s67, s67, s84\n"
      "s_lshl_b32 s84, %[xcd], 3\n"
      "s_add_u32 s67, s67, s84\n"
      "s_add_u32 s67, s67, 0xc00040\n"
      "v_and_b32 v8, 7, %[ln]\n"
      "v_lshl_add_u32 v126, v8, 9, s67\n"
      "v_mov_b32 v124, s94\n"
      "s_lshl_b32 s67, s95, 16\n"
      "v_mov_b32 v125, s67\n"
      "v_mov_b32 v120, -1\n"
      "v_mov_b32 v121, -1\n"
      "v_mov_b32 v122, -1\n"
      "v_mov_b32 v123, -1\n"
      "s_waitcnt vmcnt(13)\n"
      "s_barrier\n"
      "ds_read_b128 v[72:75], v96 offset:24576\n"
      "ds_read_b64_tr_b16 v[128:129], v102 offset:16384\n"
      "ds_read_b64_tr_b16 v[130:131], v102 offset:17408\n"
      "ds_read_b64_tr_b16 v[136:137], v102 offset:20480\n"
      "ds_read_b64_tr_b16 v[138:139], v102 offset:21504\n"
      "ds_read_b128 v[76:79], v96 offset:26624\n"
      "ds_read_b64_tr_b16 v[132:133], v103 offset:16384\n"
      "ds_read_b64_tr_b16 v[134:135], v103 offset:17408\n"
      "ds_read_b64_tr_b16 v[140:141], v103 offset:20480\n"
      "ds_read_b64_tr_b16 v[142:143], v103 offset:21504\n"
      "v_add_u32 v88, 0x38000, v88\n"
      "global_load_lds_dwordx4 v88, s[60:61] \n"
      "s_add_u32 m0, %[m0b], 91136\n"
      "s_nop 0\n"
      "global_load_lds_dwordx4 v88, s[82:83] \n"
      "s_add_u32 m0, %[m0b], 81536\n"
      "s_waitcnt vmcnt(13) lgkmcnt(4)\n"
      "s_barrier\n"
      "v_mfma_f32_16x16x32_bf16 v[32:35], v[128:131], v[72:75], 0\n"
      "ds_read_b128 v[80:83], v97 offset:24576\n"
      "ds_read_b64_tr_b16 v[144:145], v102 offset:8192\n"
      "ds_read_b64_tr_b16 v[146:147], v102 offset:9216\n"
      "v_mfma_f32_16x16x32_bf16 v[48:51], v[136:139], v[72:75], 0\n"
      "ds_read_b64_tr_b16 v[152:153], v102 offset:12288\n"
      "ds_read_b64_tr_b16 v[154:155], v102 offset:13312\n"
      "v_mfma_f32_16x16x32_bf16 v[40:43], v[128:131], v[76:79], 0\n"
      "ds_read_b128 v[84:87], v97 offset:26624\n"
      "v_mfma_f32_16x16x32_bf16 v[56:59], v[136:139], v[76:79], 0\n"
      "ds_read_b64_tr_b16 v[148:149], v103 offset:8192\n"
      "ds_read_b64_tr_b16 v[150:151], v103 offset:9216\n"
      "global_load_lds_dwordx4 v90, s[58:59] offset:384\n"
      "s_add_u32 m0, %[m0b], 82560\n"
      "s_waitcnt lgkmcnt(8)\n"
      "v_mfma_f32_16x16x32_bf16 v[36:39], v[132:135], v[72:75], 0\n"
      "ds_read_b64_tr_b16 v[156:157], v103 offset:12288\n"
      "ds_read_b64_tr_b16 v[158:159], v103 offset:13312\n"
      "v_mfma_f32_16x16x32_bf16 v[52:55], v[140:143], v[72:75], 0\n"
      "global_load_lds_dwordx4 v91, s[58:59] offset:384\n"
      "s_add_u32 m0, %[m0b], 73728\n"
      "v_mfma_f32_16x16x32_bf16 v[44:47], v[132:135], v[76:79], 0\n"
      "v_add_u32 v88, 0x38000, v88\n"
      "global_load_lds_dwordx4 v88, s[60:61] \n"
      "s_add_u32 m0, %[m0b], 74752\n"
      "v_mfma_f32_16x16x32_bf16 v[60:63], v[140:143], v[76:79], 0\n"
      "global_load_lds_dwordx4 v88, s[82:83] \n"
      "s_add_u32 m0, %[m0b], 147456\n"
      "s_waitcnt vmcnt(13) lgkmcnt(4)\n"
      "s_barrier\n"
      "v_mfma_f32_16x16x32_bf16 v[32:35], v[144:147], v[80:83], v[32:35]\n"
      "ds_read_b128 v[72:75], v96 offset:0\n"
      "ds_read_b64_tr_b16 v[128:129], v100 offset:57344\n"
      "ds_read_b64_tr_b16 v[130:131], v100 offset:58368\n"
      "v_mfma_f32_16x16x32_bf16 v[48:51], v[152:155], v[80:83], v[48:51]\n"
      "ds_read_b64_tr_b16 v[136:137], v100 offset:61440\n"
      "ds_read_b64_tr_b16 v[138:139], v100 offset:62464\n"
      "v_mfma_f32_16x16x32_bf16 v[40:43], v[144:147], v[84:87], v[40:43]\n"
      "ds_read_b128 v[76:79], v96 offset:2048\n"
      "v_mfma_f32_16x16x32_bf16 v[56:59], v[152:155], v[84:87], v[56:59]\n"
      "ds_read_b64_tr_b16 v[132:133], v101 offset:57344\n"
      "ds_read_b64_tr_b16 v[134:135], v101 offset:58368\n"
      "s_waitcnt lgkmcnt(8)\n"
      "v_mfma_f32_16x16x32_bf16 v[36:39], v[148:151], v[80:83], v[36:39]\n"
      "ds_read_b64_tr_b16 v[140:141], v101 offset:61440\n"
      "ds_read_b64_tr_b16 v[142:143], v101 offset:62464\n"
      "v_mfma_f32_16x16x32_bf16 v[52:55], v[156:159], v[80:83], v[52:55]\n"
      "v_add_u32 v88, 0x38000, v88\n"
      "global_load_lds_dwordx4 v88, s[60:61] \n"
      "s_add_u32 m0, %[m0b], 148480\n"
      "v_mfma_f32_16x16x32_bf16 v[44:47], v[148:151], v[84:87], v[44:47]\n"
      "global_load_lds_dwordx4 v88, s[82:83] \n"
      "s_add_u32 m0, %[m0b], 155648\n"
      "v_mfma_f32_16x16x32_bf16 v[60:63], v[156:159], v[84:87], v[60:63]\n"
      "s_waitcnt vmcnt(13) lgkmcnt(4)\n"
      "s_barrier\n"
      "v_mfma_f32_16x16x32_bf16 v[32:35], v[128:131], v[72:75], v[32:35]\n"
      "ds_read_b128 v[80:83], v97 offset:0\n"
      "ds_read_b64_tr_b16 v[144:145], v100 offset:49152\n"
      "ds_read_b64_tr_b16 v[146:147], v100 offset:50176\n"
      "v_mfma_f32_16x16x32_bf16 v[48:51], v[136:139], v[72:75], v[48:51]\n"
      "ds_read_b64_tr_b16 v[152:153], v100 offset:53248\n"
      "ds_read_b64_tr_b16 v[154:155], v100 offset:54272\n"
      "v_mfma_f32_16x16x32_bf16 v[40:43], v[128:131], v[76:79], v[40:43]\n"
      "ds_read_b128 v[84:87], v97 offset:2048\n"
      "v_mfma_f32_16x16x32_bf16 v[56:59], v[136:139], v[76:79], v[56:59]\n"
      "ds_read_b64_tr_b16 v[148:149], v101 offset:49152\n"
      "ds_read_b64_tr_b16 v[150:151], v101 offset:50176\n"
      "s_mov_b64 s[58:59], %[xq1]\n"
      "global_load_lds_dwordx4 v90, s[58:59] offset:0\n"
      "s_add_u32 m0, %[m0b], 156672\n"
      "s_waitcnt lgkmcnt(8)\n"
      "v_mfma_f32_16x16x32_bf16 v[36:39], v[132:135], v[72:75], v[36:39]\n"
      "ds_read_b64_tr_b16 v[156:157], v101 offset:53248\n"
      "ds_read_b64_tr_b16 v[158:159], v101 offset:54272\n"
      "v_mfma_f32_16x16x32_bf16 v[52:55], v[140:143], v[72:75], v[52:55]\n"
      "global_load_lds_dwordx4 v91, s[58:59] offset:0\n"
      "s_add_u32 m0, %[m0b], 139264\n"
      "v_mfma_f32_16x16x32_bf16 v[44:47], v[132:135], v[76:79], v[44:47]\n"
      "s_mov_b64 s[60:61], %[wq1]\n"
      "s_add_u32 s82, s60, 0x7000\n"
      "s_addc_u32 s83, s61, 0\n"
      "v_mov_b32 v88, %[voa]\n"
      "global_load_lds_dwordx4 v88, s[60:61] \n"
      "s_add_u32 m0, %[m0b], 140288\n"
      "v_mfma_f32_16x16x32_bf16 v[60:63], v[140:143], v[76:79], v[60:63]\n"
      "global_load_lds_dwordx4 v88, s[82:83] \n"
      "s_add_u32 m0, %[m0b], 122880\n"
      "s_waitcnt vmcnt(12) lgkmcnt(4)\n"
      "s_barrier\n"
      "v_mfma_f32_16x16x32_bf16 v[32:35], v[144:147], v[80:83], v[32:35]\n"
      "ds_read_b128 v[72:75], v94 offset:40960\n"
      "ds_read_b64_tr_b16 v[128:129], v100 offset:32768\n"
      "ds_read_b64_tr_b16 v[130:131], v100 offset:33792\n"
      "v_mfma_f32_16x16x32_bf16 v[48:51], v[152:155], v[80:83], v[48:51]\n"
      "ds_read_b64_tr_b16 v[136:137], v100 offset:36864\n"
      "ds_read_b64_tr_b16 v[138:139], v100 offset:37888\n"
      "v_mfma_f32_16x16x32_bf16 v[40:43], v[144:147], v[84:87], v[40:43]\n"
      "ds_read_b128 v[76:79], v94 offset:43008\n"
      "v_mfma_f32_16x16x32_bf16 v[56:59], v[152:155], v[84:87], v[56:59]\n"
      "ds_read_b64_tr_b16 v[132:133], v101 offset:32768\n"
      "ds_read_b64_tr_b16 v[134:135], v101 offset:33792\n"
      "s_waitcnt lgkmcnt(8)\n"
      "v_mfma_f32_16x16x32_bf16 v[36:39], v[148:151], v[80:83], v[36:39]\n"
      "ds_read_b64_tr_b16 v[140:141], v101 offset:36864\n"
      "ds_read_b64_tr_b16 v[142:143], v101 offset:37888\n"
      "v_mfma_f32_16x16x32_bf16 v[52:55], v[156:159], v[80:83], v[52:55]\n"
      "v_add_u32 v88, 0x38000, v88\n"
      "global_load_lds_dwordx4 v88, s[60:61] \n"
      "s_add_u32 m0, %[m0b], 123904\n"
      "v_mfma_f32_16x16x32_bf16 v[44:47], v[148:151], v[84:87], v[44:47]\n"
      "global_load_lds_dwordx4 v88, s[82:83] \n"
      "s_add_u32 m0, %[m0b], 130944\n"
      "v_mfma_f32_16x16x32_bf16 v[60:63], v[156:159], v[84:87], v[60:63]\n"
      "s_waitcnt vmcnt(12) lgkmcnt(4)\n"
      "s_barrier\n"
      "v_mfma_f32_16x16x32_bf16 v[32:35], v[128:131], v[72:75], v[32:35]\n"
      "ds_read_b128 v[80:83], v95 offset:40960\n"
      "ds_read_b64_tr_b16 v[144:145], v100 offset:24576\n"
      "ds_read_b64_tr_b16 v[146:147], v100 offset:25600\n"
      "v_mfma_f32_16x16x32_bf16 v[48:51], v[136:139], v[72:75], v[48:51]\n"
      "ds_read_b64_tr_b16 v[152:153], v100 offset:28672\n"
      "ds_read_b64_tr_b16 v[154:155], v100 offset:29696\n"
      "v_mfma_f32_16x16x32_bf16 v[40:43], v[128:131], v[76:79], v[40:43]\n"
      "ds_read_b128 v[84:87], v95 offset:43008\n"
      "v_mfma_f32_16x16x32_bf16 v[56:59], v[136:139], v[76:79], v[56:59]\n"
      "ds_read_b64_tr_b16 v[148:149], v101 offset:24576\n"
      "ds_read_b64_tr_b16 v[150:151], v101 offset:25600\n"
      "global_load_lds_dwordx4 v90, s[58:59] offset:128\n"
      "s_add_u32 m0, %[m0b], 131968\n"
      "s_waitcnt lgkmcnt(8)\n"
      "v_mfma_f32_16x16x32_bf16 v[36:39], v[132:135], v[72:75], v[36:39]\n"
      "ds_read_b64_tr_b16 v[156:157], v101 offset:28672\n"
      "ds_read_b64_tr_b16 v[158:159], v101 offset:29696\n"
      "v_mfma_f32_16x16x32_bf16 v[52:55], v[140:143], v[72:75], v[52:55]\n"
      "global_load_lds_dwordx4 v91, s[58:59] offset:128\n"
      "s_add_u32 m0, %[m0b], 114688\n"
      "v_mfma_f32_16x16x32_bf16 v[44:47], v[132:135], v[76:79], v[44:47]\n"
      "v_add_u32 v88, 0x38000, v88\n"
      "global_load_lds_dwordx4 v88, s[60:61] \n"
      "s_add_u32 m0, %[m0b], 115712\n"
      "v_mfma_f32_16x16x32_bf16 v[60:63], v[140:143], v[76:79], v[60:63]\n"
      "global_load_lds_dwordx4 v88, s[82:83] \n"
      "s_add_u32 m0, %[m0b], 98304\n"
      "s_waitcnt vmcnt(12) lgkmcnt(4)\n"
      "s_barrier\n"
      "v_mfma_f32_16x16x32_bf16 v[32:35], v[144:147], v[80:83], v[32:35]\n"
      "ds_read_b128 v[72:75], v94 offset:16384\n"
      "ds_read_b64_tr_b16 v[128:129], v100 offset:8192\n"
      "ds_read_b64_tr_b16 v[130:131], v100 offset:9216\n"
      "v_mfma_f32_16x16x32_bf16 v[48:51], v[152:155], v[80:83], v[48:51]\n"
      "ds_read_b64_tr_b16 v[136:137], v100 offset:12288\n"
      "ds_read_b64_tr_b16 v[138:139], v100 offset:13312\n"
      "v_mfma_f32_16x16x32_bf16 v[40:43], v[144:147], v[84:87], v[40:43]\n"
      "ds_read_b128 v[76:79], v94 offset:18432\n"
      "v_mfma_f32_16x16x32_bf16 v[56:59], v[152:155], v[84:87], v[56:59]\n"
      "ds_read_b64_tr_b16 v[132:133], v101 offset:8192\n"
      "ds_read_b64_tr_b16 v[134:135], v101 offset:9216\n"
      "s_waitcnt lgkmcnt(8)\n"
      "v_mfma_f32_16x16x32_bf16 v[36:39], v[148:151], v[80:83], v[36:39]\n"
      "ds_read_b64_tr_b16 v[140:141], v101 offset:12288\n"
      "ds_read_b64_tr_b16 v[142:143], v101 offset:13312\n"
      "v_mfma_f32_16x16x32_bf16 v[52:55], v[156:159], v[80:83], v[52:55]\n"
      "v_add_u32 v88, 0x38000, v88\n"
      "global_load_lds_dwordx4 v88, s[60:61] \n"
      "s_add_u32 m0, %[m0b], 99328\n"
      "v_mfma_f32_16x16x32_bf16 v[44:47], v[148:151], v[84:87], v[44:47]\n"
      "global_load_lds_dwordx4 v88, s[82:83] \n"
      "s_add_u32 m0, %[m0b], 106240\n"
      "v_mfma_f32_16x16x32_bf16 v[60:63], v[156:159], v[84:87], v[60:63]\n"
      "s_waitcnt vmcnt(12) lgkmcnt(4)\n"
      "s_barrier\n"
      "v_mfma_f32_16x16x32_bf16 v[32:35], v[128:131], v[72:75], v[32:35]\n"
      "ds_read_b128 v[80:83], v95 offset:16384\n"
      "ds_read_b64_tr_b16 v[144:145], v102 offset:16384\n"
      "ds_read_b64_tr_b16 v[146:147], v102 offset:17408\n"
      "v_mfma_f32_16x16x32_bf16 v[48:51], v[136:139], v[72:75], v[48:51]\n"
      "ds_read_b64_tr_b16 v[152:153], v102 offset:20480\n"
      "ds_read_b64_tr_b16 v[154:155], v102 offset:21504\n"
      "v_mfma_f32_16x16x32_bf16 v[40:43], v[128:131], v[76:79], v[40:43]\n"
      "ds_read_b128 v[84:87], v95 offset:18432\n"
      "v_mfma_f32_16x16x32_bf16 v[56:59], v[136:139], v[76:79], v[56:59]\n"
      "ds_read_b64_tr_b16 v[148:149], v103 offset:16384\n"
      "ds_read_b64_tr_b16 v[150:151], v103 offset:17408\n"
      "global_load_lds_dwordx4 v90, s[58:59] offset:256\n"
      "s_add_u32 m0, %[m0b], 107264\n"
      "s_waitcnt lgkmcnt(8)\n"
      "v_mfma_f32_16x16x32_bf16 v[36:39], v[132:135], v[72:75], v[36:39]\n"
      "ds_read_b64_tr_b16 v[156:157], v103 offset:20480\n"
      "ds_read_b64_tr_b16 v[158:159], v103 offset:21504\n"
      "v_mfma_f32_16x16x32_bf16 v[52:55], v[140:143], v[72:75], v[52:55]\n"
      "global_load_lds_dwordx4 v91, s[58:59] offset:256\n"
      "s_add_u32 m0, %[m0b], 90112\n"
      "v_mfma_f32_16x16x32_bf16 v[44:47], v[132:135], v[76:79], v[44:47]\n"
      "v_add_u32 v88, 0x38000, v88\n"
      "global_load_lds_dwordx4 v88, s[60:61] \n"
      "s_add_u32 m0, %[m0b], 91136\n"
      "v_mfma_f32_16x16x32_bf16 v[60:63], v[140:143], v[76:79], v[60:63]\n"
      "global_load_lds_dwordx4 v88, s[82:83] \n"
      "s_add_u32 m0, %[m0b], 73728\n"
      "s_waitcnt vmcnt(12) lgkmcnt(4)\n"
      "s_barrier\n"
      "v_mfma_f32_16x16x32_bf16 v[32:35], v[144:147], v[80:83], v[32:35]\n"
      "ds_read_b128 v[72:75], v96 offset:24576\n"
      "ds_read_b64_tr_b16 v[128:129], v102 offset:8192\n"
      "ds_read_b64_tr_b16 v[130:131], v102 offset:9216\n"
      "v_mfma_f32_16x16x32_bf16 v[48:51], v[152:155], v[80:83], v[48:51]\n"
      "ds_read_b64_tr_b16 v[136:137], v102 offset:12288\n"
      "ds_read_b64_tr_b16 v[138:139], v102 offset:13312\n"
      "v_mfma_f32_16x16x32_bf16 v[40:43], v[144:147], v[84:87], v[40:43]\n"
      "ds_read_b128 v[76:79], v96 offset:26624\n"
      "v_mfma_f32_16x16x32_bf16 v[56:59], v[152:155], v[84:87], v[56:59]\n"
      "ds_read_b64_tr_b16 v[132:133], v103 offset:8192\n"
      "ds_read_b64_tr_b16 v[134:135], v103 offset:9216\n"
      "s_waitcnt lgkmcnt(8)\n"
      "v_mfma_f32_16x16x32_bf16 v[36:39], v[148:151], v[80:83], v[36:39]\n"
      "ds_read_b64_tr_b16 v[140:141], v103 offset:12288\n"
      "ds_read_b64_tr_b16 v[142:143], v103 offset:13312\n"
      "v_mfma_f32_16x16x32_bf16 v[52:55], v[156:159], v[80:83], v[52:55]\n"
      "v_add_u32 v88, 0x38000, v88\n"
      "global_load_lds_dwordx4 v88, s[60:61] \n"
      "s_add_u32 m0, %[m0b], 74752\n"
      "v_mfma_f32_16x16x32_bf16 v[44:47], v[148:151], v[84:87], v[44:47]\n"
      "global_load_lds_dwordx4 v88, s[82:83] \n"
      "s_add_u32 m0, %[m0b], 147072\n"
      "v_mfma_f32_16x16x32_bf16 v[60:63], v[156:159], v[84:87], v[60:63]\n"
      "s_waitcnt vmcnt(12) lgkmcnt(4)\n"
      "s_barrier\n"
      "v_mfma_f32_16x16x32_bf16 v[32:35], v[128:131], v[72:75], v[32:35]\n"
      "ds_read_b128 v[80:83], v97 offset:24576\n"
      "ds_read_b64_tr_b16 v[144:145], v100 offset:57344\n"
      "ds_read_b64_tr_b16 v[146:147], v100 offset:58368\n"
      "v_mfma_f32_16x16x32_bf16 v[48:51], v[136:139], v[72:75], v[48:51]\n"
      "ds_read_b64_tr_b16 v[152:153], v100 offset:61440\n"
      "ds_read_b64_tr_b16 v[154:155], v100 offset:62464\n"
      "v_mfma_f32_16x16x32_bf16 v[40:43], v[128:131], v[76:79], v[40:43]\n"
      "ds_read_b128 v[84:87], v97 offset:26624\n"
      "v_mfma_f32_16x16x32_bf16 v[56:59], v[136:139], v[76:79], v[56:59]\n"
      "ds_read_b64_tr_b16 v[148:149], v101 offset:57344\n"
      "ds_read_b64_tr_b16 v[150:151], v101 offset:58368\n"
      "global_load_lds_dwordx4 v90, s[58:59] offset:384\n"
      "s_add_u32 m0, %[m0b], 148096\n"
      "s_waitcnt lgkmcnt(8)\n"
      "v_mfma_f32_16x16x32_bf16 v[36:39], v[132:135], v[72:75], v[36:39]\n"
      "ds_read_b64_tr_b16 v[156:157], v101 offset:61440\n"
      "ds_read_b64_tr_b16 v[158:159], v101 offset:62464\n"
      "v_mfma_f32_16x16x32_bf16 v[52:55], v[140:143], v[72:75], v[52:55]\n"
      "global_load_lds_dwordx4 v91, s[58:59] offset:384\n"
      "s_add_u32 m0, %[m0b], 81920\n"
      "v_mfma_f32_16x16x32_bf16 v[44:47], v[132:135], v[76:79], v[44:47]\n"
      "v_add_u32 v88, 0x38000, v88\n"
      "global_load_lds_dwordx4 v88, s[60:61] \n"
      "s_add_u32 m0, %[m0b], 82944\n"
      "v_mfma_f32_16x16x32_bf16 v[60:63], v[140:143], v[76:79], v[60:63]\n"
      "global_load_lds_dwordx4 v88, s[82:83] \n"
      "s_add_u32 m0, %[m0b], 139264\n"
      "s_waitcnt vmcnt(12) lgkmcnt(4)\n"
      "s_barrier\n"
      "v_mfma_f32_16x16x32_bf16 v[32:35], v[144:147], v[80:83], v[32:35]\n"
      "ds_read_b128 v[72:75], v96 offset:0\n"
      "ds_read_b64_tr_b16 v[128:129], v100 offset:49152\n"
      "ds_read_b64_tr_b16 v[130:131], v100 offset:50176\n"
      "v_mfma_f32_16x16x32_bf16 v[48:51], v[152:155], v[80:83], v[48:51]\n"
      "ds_read_b64_tr_b16 v[136:137], v100 offset:53248\n"
      "ds_read_b64_tr_b16 v[138:139], v100 offset:54272\n"
      "v_mfma_f32_16x16x32_bf16 v[40:43], v[144:147], v[84:87], v[40:43]\n"
      "ds_read_b128 v[76:79], v96 offset:2048\n"
      "v_mfma_f32_16x16x32_bf16 v[56:59], v[152:155], v[84:87], v[56:59]\n"
      "ds_read_b64_tr_b16 v[132:133], v101 offset:49152\n"
      "ds_read_b64_tr_b16 v[134:135], v101 offset:50176\n"
      "s_waitcnt lgkmcnt(8)\n"
      "v_mfma_f32_16x16x32_bf16 v[36:39], v[148:151], v[80:83], v[36:39]\n"
      "ds_read_b64_tr_b16 v[140:141], v101 offset:53248\n"
      "ds_read_b64_tr_b16 v[142:143], v101 offset:54272\n"
      "v_mfma_f32_16x16x32_bf16 v[52:55], v[156:159], v[80:83], v[52:55]\n"
      "v_add_u32 v88, 0x38000, v88\n"
      "global_load_lds_dwordx4 v88, s[60:61] \n"
      "s_add_u32 m0, %[m0b], 140288\n"
      "v_mfma_f32_16x16x32_bf16 v[44:47], v[148:151], v[84:87], v[44:47]\n"
      "global_load_lds_dwordx4 v88, s[82:83] \n"
      "s_add_u32 m0, %[m0b], 155648\n"
      "v_mfma_f32_16x16x32_bf16 v[60:63], v[156:159], v[84:87], v[60:63]\n"
      "s_waitcnt vmcnt(12) lgkmcnt(4)\n"
      "s_barrier\n"
      "v_mfma_f32_16x16x32_bf16 v[32:35], v[128:131], v[72:75], v[32:35]\n"
      "ds_read_b128 v[80:83], v97 offset:0\n"
      "ds_read_b64_tr_b16 v[144:145], v100 offset:32768\n"
      "ds_read_b64_tr_b16 v[146:147], v100 offset:33792\n"
      "v_mfma_f32_16x16x32_bf16 v[48:51], v[136:139], v[72:75], v[48:51]\n"
      "ds_read_b64_tr_b16 v[152:153], v100 offset:36864\n"
      "ds_read_b64_tr_b16 v[154:155], v100 offset:37888\n"
      "v_mfma_f32_16x16x32_bf16 v[40:43], v[128:131], v[76:79], v[40:43]\n"
      "ds_read_b128 v[84:87], v97 offset:2048\n"
      "v_mfma_f32_16x16x32_bf16 v[56:59], v[136:139], v[76:79], v[56:59]\n"
      "ds_read_b64_tr_b16 v[148:149], v101 offset:32768\n"
      "ds_read_b64_tr_b16 v[150:151], v101 offset:33792\n"
      "s_mov_b64 s[58:59], %[xq2]\n"
      "global_load_lds_dwordx4 v90, s[58:59] offset:0\n"
      "s_add_u32 m0, %[m0b], 156672\n"
      "s_waitcnt lgkmcnt(8)\n"
      "v_mfma_f32_16x16x32_bf16 v[36:39], v[132:135], v[72:75], v[36:39]\n"
      "ds_read_b64_tr_b16 v[156:157], v101 offset:36864\n"
      "ds_read_b64_tr_b16 v[158:159], v101 offset:37888\n"
      "v_mfma_f32_16x16x32_bf16 v[52:55], v[140:143], v[72:75], v[52:55]\n"
      "global_load_lds_dwordx4 v91, s[58:59] offset:0\n"
      "s_add_u32 m0, %[m0b], 122880\n"
      "v_mfma_f32_16x16x32_bf16 v[44:47], v[132:135], v[76:79], v[44:47]\n"
      "s_mov_b64 s[60:61], %[wq2]\n"
      "s_add_u32 s82, s60, 0x7000\n"
      "s_addc_u32 s83, s61, 0\n"
      "v_mov_b32 v88, %[voa]\n"
      "global_load_lds_dwordx4 v88, s[60:61] \n"
      "s_add_u32 m0, %[m0b], 123904\n"
      "v_mfma_f32_16x16x32_bf16 v[60:63], v[140:143], v[76:79], v[60:63]\n"
      "global_load_lds_dwordx4 v88, s[82:83] \n"
      "s_add_u32 m0, %[m0b], 114688\n"
      "s_waitcnt vmcnt(12) lgkmcnt(4)\n"
      "s_barrier\n"
      "v_mfma_f32_16x16x32_bf16 v[32:35], v[144:147], v[80:83], v[32:35]\n"
      "ds_read_b128 v[72:75], v94 offset:40960\n"
      "ds_read_b64_tr_b16 v[128:129], v100 offset:24576\n"
      "ds_read_b64_tr_b16 v[130:131], v100 offset:25600\n"
      "v_mfma_f32_16x16x32_bf16 v[48:51], v[152:155], v[80:83], v[48:51]\n"
      "ds_read_b64_tr_b16 v[136:137], v100 offset:28672\n"
      "ds_read_b64_tr_b16 v[138:139], v100 offset:29696\n"
      "v_mfma_f32_16x16x32_bf16 v[40:43], v[144:147], v[84:87], v[40:43]\n"
      "ds_read_b128 v[76:79], v94 offset:43008\n"
      "v_mfma_f32_16x16x32_bf16 v[56:59], v[152:155], v[84:87], v[56:59]\n"
      "ds_read_b64_tr_b16 v[132:133], v101 offset:24576\n"
      "ds_read_b64_tr_b16 v[134:135], v101 offset:25600\n"
      "s_waitcnt lgkmcnt(8)\n"
      "v_mfma_f32_16x16x32_bf16 v[36:39], v[148:151], v[80:83], v[36:39]\n"
      "ds_read_b64_tr_b16 v[140:141], v101 offset:28672\n"
      "ds_read_b64_tr_b16 v[142:143], v101 offset:29696\n"
      "v_mfma_f32_16x16x32_bf16 v[52:55], v[156:159], v[80:83], v[52:55]\n"
      "v_add_u32 v88, 0x38000, v88\n"
      "global_load_lds_dwordx4 v88, s[60:61] \n"
      "s_add_u32 m0, %[m0b], 115712\n"
      "v_mfma_f32_16x16x32_bf16 v[44:47], v[148:151], v[84:87], v[44:47]\n"
      "global_load_lds_dwordx4 v88, s[82:83] \n"
      "s_add_u32 m0, %[m0b], 130944\n"
      "v_mfma_f32_16x16x32_bf16 v[60:63], v[156:159], v[84:87], v[60:63]\n"
      "s_waitcnt vmcnt(12) lgkmcnt(4)\n"
      "s_barrier\n"
      "v_mfma_f32_16x16x32_bf16 v[32:35], v[128:131], v[72:75], v[32:35]\n"
      "ds_read_b128 v[80:83], v95 offset:40960\n"
      "ds_read_b64_tr_b16 v[144:145], v100 offset:8192\n"
      "ds_read_b64_tr_b16 v[146:147], v100 offset:9216\n"
      "v_mfma_f32_16x16x32_bf16 v[48:51], v[136:139], v[72:75], v[48:51]\n"
      "ds_read_b64_tr_b16 v[152:153], v100 offset:12288\n"
      "ds_read_b64_tr_b16 v[154:155], v100 offset:13312\n"
      "v_mfma_f32_16x16x32_bf16 v[40:43], v[128:131], v[76:79], v[40:43]\n"
      "ds_read_b128 v[84:87], v95 offset:43008\n"
      "v_mfma_f32_16x16x32_bf16 v[56:59], v[136:139], v[76:79], v[56:59]\n"
      "ds_read_b64_tr_b16 v[148:149], v101 offset:8192\n"
      "ds_read_b64_tr_b16 v[150:151], v101 offset:9216\n"
      "global_load_lds_dwordx4 v90, s[58:59] offset:128\n"
      "s_add_u32 m0, %[m0b], 131968\n"
      "s_waitcnt lgkmcnt(8)\n"
      "v_mfma_f32_16x16x32_bf16 v[36:39], v[132:135], v[72:75], v[36:39]\n"
      "ds_read_b64_tr_b16 v[156:157], v101 offset:12288\n"
      "ds_read_b64_tr_b16 v[158:159], v101 offset:13312\n"
      "v_mfma_f32_16x16x32_bf16 v[52:55], v[140:143], v[72:75], v[52:55]\n"
      "global_load_lds_dwordx4 v91, s[58:59] offset:128\n"
      "s_add_u32 m0, %[m0b], 98304\n"
      "v_mfma_f32_16x16x32_bf16 v[44:47], v[132:135], v[76:79], v[44:47]\n"
      "v_add_u32 v88, 0x38000, v88\n"
      "global_load_lds_dwordx4 v88, s[60:61] \n"
      "s_add_u32 m0, %[m0b], 99328\n"
      "v_mfma_f32_16x16x32_bf16 v[60:63], v[140:143], v[76:79], v[60:63]\n"
      "global_load_lds_dwordx4 v88, s[82:83] \n"
      "s_add_u32 m0, %[m0b], 90112\n"
      "s_waitcnt vmcnt(12) lgkmcnt(4)\n"
      "s_barrier\n"
      "v_mfma_f32_16x16x32_bf16 v[32:35], v[144:147], v[80:83], v[32:35]\n"
      "ds_read_b128 v[72:75], v96 offset:16384\n"
      "ds_read_b64_tr_b16 v[128:129], v100 offset:16384\n"
      "ds_read_b64_tr_b16 v[130:131], v100 offset:17408\n"
      "v_mfma_f32_16x16x32_bf16 v[48:51], v[152:155], v[80:83], v[48:51]\n"
      "ds_read_b64_tr_b16 v[136:137], v100 offset:20480\n"
      "ds_read_b64_tr_b16 v[138:139], v100 offset:21504\n"
      "v_mfma_f32_16x16x32_bf16 v[40:43], v[144:147], v[84:87], v[40:43]\n"
      "ds_read_b128 v[76:79], v96 offset:18432\n"
      "v_mfma_f32_16x16x32_bf16 v[56:59], v[152:155], v[84:87], v[56:59]\n"
      "ds_read_b64_tr_b16 v[132:133], v101 offset:16384\n"
      "ds_read_b64_tr_b16 v[134:135], v101 offset:17408\n"
      "s_waitcnt lgkmcnt(8)\n"
      "v_mfma_f32_16x16x32_bf16 v[36:39], v[148:151], v[80:83], v[36:39]\n"
      "ds_read_b64_tr_b16 v[140:141], v101 offset:20480\n"
      "ds_read_b64_tr_b16 v[142:143], v101 offset:21504\n"
      "v_mfma_f32_16x16x32_bf16 v[52:55], v[156:159], v[80:83], v[52:55]\n"
      "v_add_u32 v88, 0x38000, v88\n"
      "global_load_lds_dwordx4 v88, s[60:61] \n"
      "s_add_u32 m0, %[m0b], 91136\n"
      "v_mfma_f32_16x16x32_bf16 v[44:47], v[148:151], v[84:87], v[44:47]\n"
      "global_load_lds_dwordx4 v88, s[82:83] \n"
      "s_add_u32 m0, %[m0b], 106240\n"
      "v_mfma_f32_16x16x32_bf16 v[60:63], v[156:159], v[84:87], v[60:63]\n"
      "s_waitcnt vmcnt(12) lgkmcnt(4)\n"
      "s_barrier\n"
      "v_mfma_f32_16x16x32_bf16 v[32:35], v[128:131], v[72:75], v[32:35]\n"
      "ds_read_b128 v[80:83], v97 offset:16384\n"
      "ds_read_b64_tr_b16 v[144:145], v102 offset:8192\n"
      "ds_read_b64_tr_b16 v[146:147], v102 offset:9216\n"
      "v_mfma_f32_16x16x32_bf16 v[48:51], v[136:139], v[72:75], v[48:51]\n"
      "ds_read_b64_tr_b16 v[152:153], v102 offset:12288\n"
      "ds_read_b64_tr_b16 v[154:155], v102 offset:13312\n"
      "v_mfma_f32_16x16x32_bf16 v[40:43], v[128:131], v[76:79], v[40:43]\n"
      "ds_read_b128 v[84:87], v97 offset:18432\n"
      "v_mfma_f32_16x16x32_bf16 v[56:59], v[136:139], v[76:79], v[56:59]\n"
      "ds_read_b64_tr_b16 v[148:149], v103 offset:8192\n"
      "ds_read_b64_tr_b16 v[150:151], v103 offset:9216\n"
      "global_load_lds_dwordx4 v90, s[58:59] offset:256\n"
      "s_add_u32 m0, %[m0b], 107264\n"
      "s_waitcnt lgkmcnt(8)\n"
      "v_mfma_f32_16x16x32_bf16 v[36:39], v[132:135], v[72:75], v[36:39]\n"
      "ds_read_b64_tr_b16 v[156:157], v103 offset:12288\n"
      "ds_read_b64_tr_b16 v[158:159], v103 offset:13312\n"
      "v_mfma_f32_16x16x32_bf16 v[52:55], v[140:143], v[72:75], v[52:55]\n"
      "global_load_lds_dwordx4 v91, s[58:59] offset:256\n"
      "s_add_u32 m0, %[m0b], 73728\n"
      "v_mfma_f32_16x16x32_bf16 v[44:47], v[132:135], v[76:79], v[44:47]\n"
      "v_add_u32 v88, 0x38000, v88\n"
      "global_load_lds_dwordx4 v88, s[60:61] \n"
      "s_add_u32 m0, %[m0b], 74752\n"
      "v_mfma_f32_16x16x32_bf16 v[60:63], v[140:143], v[76:79], v[60:63]\n"
      "global_load_lds_dwordx4 v88, s[82:83] \n"
      "s_add_u32 m0, %[m0b], 81920\n"
      "s_waitcnt vmcnt(12) lgkmcnt(4)\n"
      "s_barrier\n"
      "v_mfma_f32_16x16x32_bf16 v[32:35], v[144:147], v[80:83], v[32:35]\n"
      "ds_read_b128 v[72:75], v96 offset:24576\n"
      "ds_read_b64_tr_b16 v[128:129], v100 offset:57344\n"
      "ds_read_b64_tr_b16 v[130:131], v100 offset:58368\n"
      "v_mfma_f32_16x16x32_bf16 v[48:51], v[152:155], v[80:83], v[48:51]\n"
      "ds_read_b64_tr_b16 v[136:137], v100 offset:61440\n"
      "ds_read_b64_tr_b16 v[138:139], v100 offset:62464\n"
      "v_mfma_f32_16x16x32_bf16 v[40:43], v[144:147], v[84:87], v[40:43]\n"
      "ds_read_b128 v[76:79], v96 offset:26624\n"
      "v_mfma_f32_16x16x32_bf16 v[56:59], v[152:155], v[84:87], v[56:59]\n"
      "ds_read_b64_tr_b16 v[132:133], v101 offset:57344\n"
      "ds_read_b64_tr_b16 v[134:135], v101 offset:58368\n"
      "s_waitcnt lgkmcnt(8)\n"
      "v_mfma_f32_16x16x32_bf16 v[36:39], v[148:151], v[80:83], v[36:39]\n"
      "ds_read_b64_tr_b16 v[140:141], v101 offset:61440\n"
      "ds_read_b64_tr_b16 v[142:143], v101 offset:62464\n"
      "v_mfma_f32_16x16x32_bf16 v[52:55], v[156:159], v[80:83], v[52:55]\n"
      "v_add_u32 v88, 0x38000, v88\n"
      "global_load_lds_dwordx4 v88, s[60:61] \n"
      "s_add_u32 m0, %[m0b], 82944\n"
      "v_mfma_f32_16x16x32_bf16 v[44:47], v[148:151], v[84:87], v[44:47]\n"
      "global_load_lds_dwordx4 v88, s[82:83] \n"
      "s_add_u32 m0, %[m0b], 147072\n"
      "v_mfma_f32_16x16x32_bf16 v[60:63], v[156:159], v[84:87], v[60:63]\n"
      "s_waitcnt vmcnt(12) lgkmcnt(4)\n"
      "s_barrier\n"
      "v_mfma_f32_16x16x32_bf16 v[32:35], v[128:131], v[72:75], v[32:35]\n"
      "ds_read_b128 v[80:83], v97 offset:24576\n"
      "ds_read_b64_tr_b16 v[144:145], v100 offset:49152\n"
      "ds_read_b64_tr_b16 v[146:147], v100 offset:50176\n"
      "v_mfma_f32_16x16x32_bf16 v[48:51], v[136:139], v[72:75], v[48:51]\n"
      "ds_read_b64_tr_b16 v[152:153], v100 offset:53248\n"
      "ds_read_b64_tr_b16 v[154:155], v100 offset:54272\n"
      "v_mfma_f32_16x16x32_bf16 v[40:43], v[128:131], v[76:79], v[40:43]\n"
      "ds_read_b128 v[84:87], v97 offset:26624\n"
      "v_mfma_f32_16x16x32_bf16 v[56:59], v[136:139], v[76:79], v[56:59]\n"
      "ds_read_b64_tr_b16 v[148:149], v101 offset:49152\n"
      "ds_read_b64_tr_b16 v[150:151], v101 offset:50176\n"
      "global_load_lds_dwordx4 v90, s[58:59] offset:384\n"
      "s_add_u32 m0, %[m0b], 148096\n"
      "s_waitcnt lgkmcnt(8)\n"
      "v_mfma_f32_16x16x32_bf16 v[36:39], v[132:135], v[72:75], v[36:39]\n"
      "ds_read_b64_tr_b16 v[156:157], v101 offset:53248\n"
      "ds_read_b64_tr_b16 v[158:159], v101 offset:54272\n"
      "v_mfma_f32_16x16x32_bf16 v[52:55], v[140:143], v[72:75], v[52:55]\n"
      "global_load_lds_dwordx4 v91, s[58:59] offset:384\n"
      "s_add_u32 m0, %[m0b], 139264\n"
      "v_mfma_f32_16x16x32_bf16 v[44:47], v[132:135], v[76:79], v[44:47]\n"
      "v_add_u32 v88, 0x38000, v88\n"
      "global_load_lds_dwordx4 v88, s[60:61] \n"
      "s_add_u32 m0, %[m0b], 140288\n"
      "v_mfma_f32_16x16x32_bf16 v[60:63], v[140:143], v[76:79], v[60:63]\n"
      "global_load_lds_dwordx4 v88, s[82:83] \n"
      "s_add_u32 m0, %[m0b], 122880\n"
      "s_waitcnt vmcnt(12) lgkmcnt(4)\n"
      "s_barrier\n"
      "v_mfma_f32_16x16x32_bf16 v[32:35], v[144:147], v[80:83], v[32:35]\n"
      "ds_read_b128 v[72:75], v96 offset:0\n"
      "ds_read_b64_tr_b16 v[128:129], v100 offset:32768\n"
      "ds_read_b64_tr_b16 v[130:131], v100 offset:33792\n"
      "v_mfma_f32_16x16x32_bf16 v[48:51], v[152:155], v[80:83], v[48:51]\n"
      "ds_read_b64_tr_b16 v[136:137], v100 offset:36864\n"
      "ds_read_b64_tr_b16 v[138:139], v100 offset:37888\n"
      "v_mfma_f32_16x16x32_bf16 v[40:43], v[144:147], v[84:87], v[40:43]\n"
      "ds_read_b128 v[76:79], v96 offset:2048\n"
      "v_mfma_f32_16x16x32_bf16 v[56:59], v[152:155], v[84:87], v[56:59]\n"
      "ds_read_b64_tr_b16 v[132:133], v101 offset:32768\n"
      "ds_read_b64_tr_b16 v[134:135], v101 offset:33792\n"
      "s_waitcnt lgkmcnt(8)\n"
      "v_mfma_f32_16x16x32_bf16 v[36:39], v[148:151], v[80:83], v[36:39]\n"
      "ds_read_b64_tr_b16 v[140:141], v101 offset:36864\n"
      "ds_read_b64_tr_b16 v[142:143], v101 offset:37888\n"
      "v_mfma_f32_16x16x32_bf16 v[52:55], v[156:159], v[80:83], v[52:55]\n"
      "v_add_u32 v88, 0x38000, v88\n"
      "global_load_lds_dwordx4 v88, s[60:61] \n"
      "s_add_u32 m0, %[m0b], 123904\n"
      "v_mfma_f32_16x16x32_bf16 v[44:47], v[148:151], v[84:87], v[44:47]\n"
      "global_load_lds_dwordx4 v88, s[82:83] \n"
      "s_add_u32 m0, %[m0b], 155648\n"
      "v_mfma_f32_16x16x32_bf16 v[60:63], v[156:159], v[84:87], v[60:63]\n"
      "s_waitcnt vmcnt(12) lgkmcnt(4)\n"
      "s_barrier\n"
      "v_mfma_f32_16x16x32_bf16 v[32:35], v[128:131], v[72:75], v[32:35]\n"
      "ds_read_b128 v[80:83], v97 offset:0\n"
      "ds_read_b64_tr_b16 v[144:145], v100 offset:24576\n"
      "ds_read_b64_tr_b16 v[146:147], v100 offset:25600\n"
      "v_mfma_f32_16x16x32_bf16 v[48:51], v[136:139], v[72:75], v[48:51]\n"
      "ds_read_b64_tr_b16 v[152:153], v100 offset:28672\n"
      "ds_read_b64_tr_b16 v[154:155], v100 offset:29696\n"
      "v_mfma_f32_16x16x32_bf16 v[40:43], v[128:131], v[76:79], v[40:43]\n"
      "ds_read_b128 v[84:87], v97 offset:2048\n"
      "v_mfma_f32_16x16x32_bf16 v[56:59], v[136:139], v[76:79], v[56:59]\n"
      "ds_read_b64_tr_b16 v[148:149], v101 offset:24576\n"
      "ds_read_b64_tr_b16 v[150:151], v101 offset:25600\n"
      "s_mov_b64 s[58:59], %[xq3]\n"
      "global_load_lds_dwordx4 v90, s[58:59] offset:0\n"
      "s_add_u32 m0, %[m0b], 156672\n"
      "s_waitcnt lgkmcnt(8)\n"
      "v_mfma_f32_16x16x32_bf16 v[36:39], v[132:135], v[72:75], v[36:39]\n"
      "ds_read_b64_tr_b16 v[156:157], v101 offset:28672\n"
      "ds_read_b64_tr_b16 v[158:159], v101 offset:29696\n"
      "v_mfma_f32_16x16x32_bf16 v[52:55], v[140:143], v[72:75], v[52:55]\n"
      "global_load_lds_dwordx4 v91, s[58:59] offset:0\n"
      "s_add_u32 m0, %[m0b], 114688\n"
      "v_mfma_f32_16x16x32_bf16 v[44:47], v[132:135], v[76:79], v[44:47]\n"
      "s_mov_b64 s[60:61], %[wq3]\n"
      "s_add_u32 s82, s60, 0x7000\n"
      "s_addc_u32 s83, s61, 0\n"
      "v_mov_b32 v88, %[voa]\n"
      "global_load_lds_dwordx4 v88, s[60:61] \n"
      "s_add_u32 m0, %[m0b], 115712\n"
      "v_mfma_f32_16x16x32_bf16 v[60:63], v[140:143], v[76:79], v[60:63]\n"
      "global_load_lds_dwordx4 v88, s[82:83] \n"
      "s_add_u32 m0, %[m0b], 98304\n"
      "s_waitcnt vmcnt(12) lgkmcnt(4)\n"
      "s_barrier\n"
      "v_mfma_f32_16x16x32_bf16 v[32:35], v[144:147], v[80:83], v[32:35]\n"
      "ds_read_b128 v[72:75], v94 offset:40960\n"
      "ds_read_b64_tr_b16 v[128:129], v100 offset:8192\n"
      "ds_read_b64_tr_b16 v[130:131], v100 offset:9216\n"
      "v_mfma_f32_16x16x32_bf16 v[48:51], v[152:155], v[80:83], v[48:51]\n"
      "ds_read_b64_tr_b16 v[136:137], v100 offset:12288\n"
      "ds_read_b64_tr_b16 v[138:139], v100 offset:13312\n"
      "v_mfma_f32_16x16x32_bf16 v[40:43], v[144:147], v[84:87], v[40:43]\n"
      "ds_read_b128 v[76:79], v94 offset:43008\n"
      "v_mfma_f32_16x16x32_bf16 v[56:59], v[152:155], v[84:87], v[56:59]\n"
      "ds_read_b64_tr_b16 v[132:133], v101 offset:8192\n"
      "ds_read_b64_tr_b16 v[134:135], v101 offset:9216\n"
      "s_waitcnt lgkmcnt(8)\n"
      "v_mfma_f32_16x16x32_bf16 v[36:39], v[148:151], v[80:83], v[36:39]\n"
      "ds_read_b64_tr_b16 v[140:141], v101 offset:12288\n"
      "ds_read_b64_tr_b16 v[142:143], v101 offset:13312\n"
      "v_mfma_f32_16x16x32_bf16 v[52:55], v[156:159], v[80:83], v[52:55]\n"
      "v_add_u32 v88, 0x38000, v88\n"
      "global_load_lds_dwordx4 v88, s[60:61] \n"
      "s_add_u32 m0, %[m0b], 99328\n"
      "v_mfma_f32_16x16x32_bf16 v[44:47], v[148:151], v[84:87], v[44:47]\n"
      "global_load_lds_dwordx4 v88, s[82:83] \n"
      "s_add_u32 m0, %[m0b], 130944\n"
      "v_mfma_f32_16x16x32_bf16 v[60:63], v[156:159], v[84:87], v[60:63]\n"
      "s_waitcnt vmcnt(12) lgkmcnt(4)\n"
      "s_barrier\n"
      "v_mfma_f32_16x16x32_bf16 v[32:35], v[128:131], v[72:75], v[32:35]\n"
      "ds_read_b128 v[80:83], v95 offset:40960\n"
      "ds_read_b64_tr_b16 v[144:145], v100 offset:16384\n"
      "ds_read_b64_tr_b16 v[146:147], v100 offset:17408\n"
      "v_mfma_f32_16x16x32_bf16 v[48:51], v[136:139], v[72:75], v[48:51]\n"
      "ds_read_b64_tr_b16 v[152:153], v100 offset:20480\n"
      "ds_read_b64_tr_b16 v[154:155], v100 offset:21504\n"
      "v_mfma_f32_16x16x32_bf16 v[40:43], v[128:131], v[76:79], v[40:43]\n"
      "ds_read_b128 v[84:87], v95 offset:43008\n"
      "v_mfma_f32_16x16x32_bf16 v[56:59], v[136:139], v[76:79], v[56:59]\n"
      "ds_read_b64_tr_b16 v[148:149], v101 offset:16384\n"
      "ds_read_b64_tr_b16 v[150:151], v101 offset:17408\n"
      "global_load_lds_dwordx4 v90, s[58:59] offset:128\n"
      "s_add_u32 m0, %[m0b], 131968\n"
      "s_waitcnt lgkmcnt(8)\n"
      "v_mfma_f32_16x16x32_bf16 v[36:39], v[132:135], v[72:75], v[36:39]\n"
      "ds_read_b64_tr_b16 v[156:157], v101 offset:20480\n"
      "ds_read_b64_tr_b16 v[158:159], v101 offset:21504\n"
      "v_mfma_f32_16x16x32_bf16 v[52:55], v[140:143], v[72:75], v[52:55]\n"
      "global_load_lds_dwordx4 v91, s[58:59] offset:128\n"
      "s_add_u32 m0, %[m0b], 90112\n"
      "v_mfma_f32_16x16x32_bf16 v[44:47], v[132:135], v[76:79], v[44:47]\n"
      "v_add_u32 v88, 0x38000, v88\n"
      "global_load_lds_dwordx4 v88, s[60:61] \n"
      "s_add_u32 m0, %[m0b], 91136\n"
      "v_mfma_f32_16x16x32_bf16 v[60:63], v[140:143], v[76:79], v[60:63]\n"
      "global_load_lds_dwordx4 v88, s[82:83] \n"
      "s_add_u32 m0, %[m0b], 73728\n"
      "s_waitcnt vmcnt(12) lgkmcnt(4)\n"
      "s_barrier\n"
      "v_mfma_f32_16x16x32_bf16 v[32:35], v[144:147], v[80:83], v[32:35]\n"
      "ds_read_b128 v[72:75], v96 offset:16384\n"
      "ds_read_b64_tr_b16 v[128:129], v102 offset:8192\n"
      "ds_read_b64_tr_b16 v[130:131], v102 offset:9216\n"
      "v_mfma_f32_16x16x32_bf16 v[48:51], v[152:155], v[80:83], v[48:51]\n"
      "ds_read_b64_tr_b16 v[136:137], v102 offset:12288\n"
      "ds_read_b64_tr_b16 v[138:139], v102 offset:13312\n"
      "v_mfma_f32_16x16x32_bf16 v[40:43], v[144:147], v[84:87], v[40:43]\n"
      "ds_read_b128 v[76:79], v96 offset:18432\n"
      "v_mfma_f32_16x16x32_bf16 v[56:59], v[152:155], v[84:87], v[56:59]\n"
      "ds_read_b64_tr_b16 v[132:133], v103 offset:8192\n"
      "ds_read_b64_tr_b16 v[134:135], v103 offset:9216\n"
      "s_waitcnt lgkmcnt(8)\n"
      "v_mfma_f32_16x16x32_bf16 v[36:39], v[148:151], v[80:83], v[36:39]\n"
      "ds_read_b64_tr_b16 v[140:141], v103 offset:12288\n"
      "ds_read_b64_tr_b16 v[142:143], v103 offset:13312\n"
      "v_mfma_f32_16x16x32_bf16 v[52:55], v[156:159], v[80:83], v[52:55]\n"
      "v_add_u32 v88, 0x38000, v88\n"
      "global_load_lds_dwordx4 v88, s[60:61] \n"
      "s_add_u32 m0, %[m0b], 74752\n"
      "v_mfma_f32_16x16x32_bf16 v[44:47], v[148:151], v[84:87], v[44:47]\n"
      "global_load_lds_dwordx4 v88, s[82:83] \n"
      "s_add_u32 m0, %[m0b], 106240\n"
      "v_mfma_f32_16x16x32_bf16 v[60:63], v[156:159], v[84:87], v[60:63]\n"
      "s_waitcnt vmcnt(12) lgkmcnt(4)\n"
      "s_barrier\n"
      "v_mfma_f32_16x16x32_bf16 v[32:35], v[128:131], v[72:75], v[32:35]\n"
      "ds_read_b128 v[80:83], v97 offset:16384\n"
      "ds_read_b64_tr_b16 v[144:145], v100 offset:57344\n"
      "ds_read_b64_tr_b16 v[146:147], v100 offset:58368\n"
      "v_mfma_f32_16x16x32_bf16 v[48:51], v[136:139], v[72:75], v[48:51]\n"
      "ds_read_b64_tr_b16 v[152:153], v100 offset:61440\n"
      "ds_read_b64_tr_b16 v[154:155], v100 offset:62464\n"
      "v_mfma_f32_16x16x32_bf16 v[40:43], v[128:131], v[76:79], v[40:43]\n"
      "ds_read_b128 v[84:87], v97 offset:18432\n"
      "v_mfma_f32_16x16x32_bf16 v[56:59], v[136:139], v[76:79], v[56:59]\n"
      "ds_read_b64_tr_b16 v[148:149], v101 offset:57344\n"
      "ds_read_b64_tr_b16 v[150:151], v101 offset:58368\n"
      "global_load_lds_dwordx4 v90, s[58:59] offset:256\n"
      "s_add_u32 m0, %[m0b], 107264\n"
      "s_waitcnt lgkmcnt(8)\n"
      "v_mfma_f32_16x16x32_bf16 v[36:39], v[132:135], v[72:75], v[36:39]\n"
      "ds_read_b64_tr_b16 v[156:157], v101 offset:61440\n"
      "ds_read_b64_tr_b16 v[158:159], v101 offset:62464\n"
      "v_mfma_f32_16x16x32_bf16 v[52:55], v[140:143], v[72:75], v[52:55]\n"
      "global_load_lds_dwordx4 v91, s[58:59] offset:256\n"
      "s_add_u32 m0, %[m0b], 81920\n"
      "v_mfma_f32_16x16x32_bf16 v[44:47], v[132:135], v[76:79], v[44:47]\n"
      "v_add_u32 v88, 0x38000, v88\n"
      "global_load_lds_dwordx4 v88, s[60:61] \n"
      "s_add_u32 m0, %[m0b], 82944\n"
      "v_mfma_f32_16x16x32_bf16 v[60:63], v[140:143], v[76:79], v[60:63]\n"
      "global_load_lds_dwordx4 v88, s[82:83] \n"
      "s_add_u32 m0, %[m0b], 65536\n"
      "s_waitcnt vmcnt(12) lgkmcnt(4)\n"
      "s_barrier\n"
      "v_mfma_f32_16x16x32_bf16 v[32:35], v[144:147], v[80:83], v[32:35]\n"
      "ds_read_b128 v[72:75], v96 offset:24576\n"
      "ds_read_b64_tr_b16 v[128:129], v100 offset:49152\n"
      "ds_read_b64_tr_b16 v[130:131], v100 offset:50176\n"
      "v_mfma_f32_16x16x32_bf16 v[48:51], v[152:155], v[80:83], v[48:51]\n"
      "ds_read_b64_tr_b16 v[136:137], v100 offset:53248\n"
      "ds_read_b64_tr_b16 v[138:139], v100 offset:54272\n"
      "v_mfma_f32_16x16x32_bf16 v[40:43], v[144:147], v[84:87], v[40:43]\n"
      "ds_read_b128 v[76:79], v96 offset:26624\n"
      "v_mfma_f32_16x16x32_bf16 v[56:59], v[152:155], v[84:87], v[56:59]\n"
      "ds_read_b64_tr_b16 v[132:133], v101 offset:49152\n"
      "ds_read_b64_tr_b16 v[134:135], v101 offset:50176\n"
      "s_waitcnt lgkmcnt(8)\n"
      "v_mfma_f32_16x16x32_bf16 v[36:39], v[148:151], v[80:83], v[36:39]\n"
      "ds_read_b64_tr_b16 v[140:141], v101 offset:53248\n"
      "ds_read_b64_tr_b16 v[142:143], v101 offset:54272\n"
      "v_mfma_f32_16x16x32_bf16 v[52:55], v[156:159], v[80:83], v[52:55]\n"
      "v_add_u32 v88, 0x38000, v88\n"
      "global_load_lds_dwordx4 v88, s[60:61] \n"
      "s_add_u32 m0, %[m0b], 66560\n"
      "v_mfma_f32_16x16x32_bf16 v[44:47], v[148:151], v[84:87], v[44:47]\n"
      "global_load_lds_dwordx4 v88, s[82:83] \n"
      "s_add_u32 m0, %[m0b], 56960\n"
      "v_mfma_f32_16x16x32_bf16 v[60:63], v[156:159], v[84:87], v[60:63]\n"
      "s_waitcnt vmcnt(12) lgkmcnt(4)\n"
      "s_barrier\n"
      "v_mfma_f32_16x16x32_bf16 v[32:35], v[128:131], v[72:75], v[32:35]\n"
      "ds_read_b128 v[80:83], v97 offset:24576\n"
      "ds_read_b64_tr_b16 v[144:145], v100 offset:32768\n"
      "ds_read_b64_tr_b16 v[146:147], v100 offset:33792\n"
      "v_mfma_f32_16x16x32_bf16 v[48:51], v[136:139], v[72:75], v[48:51]\n"
      "ds_read_b64_tr_b16 v[152:153], v100 offset:36864\n"
      "ds_read_b64_tr_b16 v[154:155], v100 offset:37888\n"
      "v_mfma_f32_16x16x32_bf16 v[40:43], v[128:131], v[76:79], v[40:43]\n"
      "ds_read_b128 v[84:87], v97 offset:26624\n"
      "v_mfma_f32_16x16x32_bf16 v[56:59], v[136:139], v[76:79], v[56:59]\n"
      "ds_read_b64_tr_b16 v[148:149], v101 offset:32768\n"
      "ds_read_b64_tr_b16 v[150:151], v101 offset:33792\n"
      "global_load_lds_dwordx4 v90, s[58:59] offset:384\n"
      "s_add_u32 m0, %[m0b], 57984\n"
      "s_waitcnt lgkmcnt(8)\n"
      "v_mfma_f32_16x16x32_bf16 v[36:39], v[132:135], v[72:75], v[36:39]\n"
      "ds_read_b64_tr_b16 v[156:157], v101 offset:36864\n"
      "ds_read_b64_tr_b16 v[158:159], v101 offset:37888\n"
      "v_mfma_f32_16x16x32_bf16 v[52:55], v[140:143], v[72:75], v[52:55]\n"
      "global_load_lds_dwordx4 v91, s[58:59] offset:384\n"
      "s_add_u32 m0, %[m0b], 49152\n"
      "v_mfma_f32_16x16x32_bf16 v[44:47], v[132:135], v[76:79], v[44:47]\n"
      "v_add_u32 v88, 0x38000, v88\n"
      "global_load_lds_dwordx4 v88, s[60:61] \n"
      "s_add_u32 m0, %[m0b], 50176\n"
      "v_mfma_f32_16x16x32_bf16 v[60:63], v[140:143], v[76:79], v[60:63]\n"
      "global_load_lds_dwordx4 v88, s[82:83] \n"
      "s_add_u32 m0, %[m0b], 40960\n"
      "s_waitcnt vmcnt(12) lgkmcnt(4)\n"
      "s_barrier\n"
      "v_mfma_f32_16x16x32_bf16 v[32:35], v[144:147], v[80:83], v[32:35]\n"
      "ds_read_b128 v[72:75], v96 offset:0\n"
      "ds_read_b64_tr_b16 v[128:129], v100 offset:24576\n"
      "ds_read_b64_tr_b16 v[130:131], v100 offset:25600\n"
      "v_mfma_f32_16x16x32_bf16 v[48:51], v[152:155], v[80:83], v[48:51]\n"
      "ds_read_b64_tr_b16 v[136:137], v100 offset:28672\n"
      "ds_read_b64_tr_b16 v[138:139], v100 offset:29696\n"
      "v_mfma_f32_16x16x32_bf16 v[40:43], v[144:147], v[84:87], v[40:43]\n"
      "ds_read_b128 v[76:79], v96 offset:2048\n"
      "v_mfma_f32_16x16x32_bf16 v[56:59], v[152:155], v[84:87], v[56:59]\n"
      "ds_read_b64_tr_b16 v[132:133], v101 offset:24576\n"
      "ds_read_b64_tr_b16 v[134:135], v101 offset:25600\n"
      "v_add_u32 v88, 0x38000, v88\n"
      "global_load_lds_dwordx4 v88, s[60:61] \n"
      "s_add_u32 m0, %[m0b], 41984\n"
      "s_waitcnt lgkmcnt(8)\n"
      "v_mfma_f32_16x16x32_bf16 v[36:39], v[148:151], v[80:83], v[36:39]\n"
      "ds_read_b64_tr_b16 v[140:141], v101 offset:28672\n"
      "ds_read_b64_tr_b16 v[142:143], v101 offset:29696\n"
      "v_mfma_f32_16x16x32_bf16 v[52:55], v[156:159], v[80:83], v[52:55]\n"
      "global_load_lds_dwordx4 v88, s[82:83] \n"
      "s_add_u32 m0, %[m0b], 155648\n"
      "v_mfma_f32_16x16x32_bf16 v[44:47], v[148:151], v[84:87], v[44:47]\n"
      "global_load_dwordx2 v[6:7], v5, %[ws] sc1\n"
      "v_mfma_f32_16x16x32_bf16 v[60:63], v[156:159], v[84:87], v[60:63]\n"
      "s_waitcnt vmcnt(13) lgkmcnt(4)\n"
      "s_barrier\n"
      "v_mfma_f32_16x16x32_bf16 v[32:35], v[128:131], v[72:75], v[32:35]\n"
      "ds_read_b128 v[80:83], v97 offset:0\n"
      "ds_read_b64_tr_b16 v[144:145], v100 offset:8192\n"
      "ds_read_b64_tr_b16 v[146:147], v100 offset:9216\n"
      "v_mfma_f32_16x16x32_bf16 v[48:51], v[136:139], v[72:75], v[48:51]\n"
      "ds_read_b64_tr_b16 v[152:153], v100 offset:12288\n"
      "ds_read_b64_tr_b16 v[154:155], v100 offset:13312\n"
      "v_mfma_f32_16x16x32_bf16 v[40:43], v[128:131], v[76:79], v[40:43]\n"
      "ds_read_b128 v[84:87], v97 offset:2048\n"
      "v_mfma_f32_16x16x32_bf16 v[56:59], v[136:139], v[76:79], v[56:59]\n"
      "ds_read_b64_tr_b16 v[148:149], v101 offset:8192\n"
      "ds_read_b64_tr_b16 v[150:151], v101 offset:9216\n"
      "s_add_u32 s88, s44, s56\n"
      "s_addc_u32 s89, s45, 0\n"
      "s_add_u32 s56, s56, 0x10000\n"
      "s_cmp_eq_u32 s56, 0xe0000\n"
      "s_cselect_b32 s56, 0, s56\n"
      "global_load_lds_dwordx4 v9, s[88:89]\n"
      "s_add_u32 m0, %[m0b], 156672\n"
      "s_waitcnt lgkmcnt(8)\n"
      "v_mfma_f32_16x16x32_bf16 v[36:39], v[132:135], v[72:75], v[36:39]\n"
      "ds_read_b64_tr_b16 v[156:157], v101 offset:12288\n"
      "ds_read_b64_tr_b16 v[158:159], v101 offset:13312\n"
      "v_mfma_f32_16x16x32_bf16 v[52:55], v[140:143], v[72:75], v[52:55]\n"
      "s_add_u32 s88, s88, 0x8000\n"
      "s_addc_u32 s89, s89, 0\n"
      "global_load_lds_dwordx4 v9, s[88:89]\n"
      "s_add_u32 m0, %[m0b], 147456\n"
      "v_mfma_f32_16x16x32_bf16 v[44:47], v[132:135], v[76:79], v[44:47]\n"
      "s_add_u32 s88, s44, s56\n"
      "s_addc_u32 s89, s45, 0\n"
      "s_add_u32 s56, s56, 0x10000\n"
      "s_cmp_eq_u32 s56, 0xe0000\n"
      "s_cselect_b32 s56, 0, s56\n"
      "global_load_lds_dwordx4 v9, s[88:89]\n"
      "s_add_u32 m0, %[m0b], 148480\n"
      "v_mfma_f32_16x16x32_bf16 v[60:63], v[140:143], v[76:79], v[60:63]\n"
      "s_waitcnt vmcnt(12) lgkmcnt(4)\n"
      "s_barrier\n"
      "v_mfma_f32_16x16x32_bf16 v[32:35], v[144:147], v[80:83], v[32:35]\n"
      "ds_read_b128 v[72:75], v94 offset:40960\n"
      "ds_read_b64_tr_b16 v[128:129], v100 offset:16384\n"
      "ds_read_b64_tr_b16 v[130:131], v100 offset:17408\n"
      "v_mfma_f32_16x16x32_bf16 v[48:51], v[152:155], v[80:83], v[48:51]\n"
      "ds_read_b64_tr_b16 v[136:137], v100 offset:20480\n"
      "ds_read_b64_tr_b16 v[138:139], v100 offset:21504\n"
      "v_mfma_f32_16x16x32_bf16 v[40:43], v[144:147], v[84:87], v[40:43]\n"
      "ds_read_b128 v[76:79], v94 offset:43008\n"
      "v_mfma_f32_16x16x32_bf16 v[56:59], v[152:155], v[84:87], v[56:59]\n"
      "ds_read_b64_tr_b16 v[132:133], v101 offset:16384\n"
      "ds_read_b64_tr_b16 v[134:135], v101 offset:17408\n"
      "s_add_u32 s88, s88, 0x8000\n"
      "s_addc_u32 s89, s89, 0\n"
      "global_load_lds_dwordx4 v9, s[88:89]\n"
      "s_add_u32 m0, %[m0b], 139264\n"
      "s_waitcnt lgkmcnt(8)\n"
      "v_mfma_f32_16x16x32_bf16 v[36:39], v[148:151], v[80:83], v[36:39]\n"
      "ds_read_b64_tr_b16 v[140:141], v101 offset:20480\n"
      "ds_read_b64_tr_b16 v[142:143], v101 offset:21504\n"
      "v_mfma_f32_16x16x32_bf16 v[52:55], v[156:159], v[80:83], v[52:55]\n"
      "s_add_u32 s88, s44, s56\n"
      "s_addc_u32 s89, s45, 0\n"
      "s_add_u32 s56, s56, 0x10000\n"
      "s_cmp_eq_u32 s56, 0xe0000\n"
      "s_cselect_b32 s56, 0, s56\n"
      "global_load_lds_dwordx4 v9, s[88:89]\n"
      "s_add_u32 m0, %[m0b], 140288\n"
      "v_mfma_f32_16x16x32_bf16 v[44:47], v[148:151], v[84:87], v[44:47]\n"
      "s_add_u32 s88, s88, 0x8000\n"
      "s_addc_u32 s89, s89, 0\n"
      "global_load_lds_dwordx4 v9, s[88:89]\n"
      "s_add_u32 m0, %[m0b], 131072\n"
      "v_mfma_f32_16x16x32_bf16 v[60:63], v[156:159], v[84:87], v[60:63]\n"
      "s_waitcnt vmcnt(13) lgkmcnt(4)\n"
      "s_barrier\n"
      "v_mfma_f32_16x16x32_bf16 v[32:35], v[128:131], v[72:75], v[32:35]\n"
      "ds_read_b128 v[80:83], v95 offset:40960\n"
      "ds_read_b64_tr_b16 v[144:145], v100 offset:0\n"
      "ds_read_b64_tr_b16 v[146:147], v100 offset:1024\n"
      "v_mfma_f32_16x16x32_bf16 v[48:51], v[136:139], v[72:75], v[48:51]\n"
      "ds_read_b64_tr_b16 v[152:153], v100 offset:4096\n"
      "ds_read_b64_tr_b16 v[154:155], v100 offset:5120\n"
      "v_mfma_f32_16x16x32_bf16 v[40:43], v[128:131], v[76:79], v[40:43]\n"
      "ds_read_b128 v[84:87], v95 offset:43008\n"
      "v_mfma_f32_16x16x32_bf16 v[56:59], v[136:139], v[76:79], v[56:59]\n"
      "ds_read_b64_tr_b16 v[148:149], v101 offset:0\n"
      "ds_read_b64_tr_b16 v[150:151], v101 offset:1024\n"
      "s_add_u32 s88, s44, s56\n"
      "s_addc_u32 s89, s45, 0\n"
      "s_add_u32 s56, s56, 0x10000\n"
      "s_cmp_eq_u32 s56, 0xe0000\n"
      "s_cselect_b32 s56, 0, s56\n"
      "global_load_lds_dwordx4 v9, s[88:89]\n"
      "s_add_u32 m0, %[m0b], 132096\n"
      "s_waitcnt lgkmcnt(8)\n"
      "v_mfma_f32_16x16x32_bf16 v[36:39], v[132:135], v[72:75], v[36:39]\n"
      "ds_read_b64_tr_b16 v[156:157], v101 offset:4096\n"
      "ds_read_b64_tr_b16 v[158:159], v101 offset:5120\n"
      "v_mfma_f32_16x16x32_bf16 v[52:55], v[140:143], v[72:75], v[52:55]\n"
      "s_add_u32 s88, s88, 0x8000\n"
      "s_addc_u32 s89, s89, 0\n"
      "global_load_lds_dwordx4 v9, s[88:89]\n"
      "s_add_u32 m0, %[m0b], 122880\n"
      "v_mfma_f32_16x16x32_bf16 v[44:47], v[132:135], v[76:79], v[44:47]\n"
      "s_add_u32 s88, s44, s56\n"
      "s_addc_u32 s89, s45, 0\n"
      "s_add_u32 s56, s56, 0x10000\n"
      "s_cmp_eq_u32 s56, 0xe0000\n"
      "s_cselect_b32 s56, 0, s56\n"
      "global_load_lds_dwordx4 v9, s[88:89]\n"
      "s_add_u32 m0, %[m0b], 123904\n"
      "v_mfma_f32_16x16x32_bf16 v[60:63], v[140:143], v[76:79], v[60:63]\n"
      "s_waitcnt vmcnt(12) lgkmcnt(4)\n"
      "s_barrier\n"
      "v_mfma_f32_16x16x32_bf16 v[32:35], v[144:147], v[80:83], v[32:35]\n"
      "ds_read_b128 v[72:75], v92 offset:57344\n"
      "ds_read_b64_tr_b16 v[128:129], v98 offset:49152\n"
      "ds_read_b64_tr_b16 v[130:131], v98 offset:50176\n"
      "v_mfma_f32_16x16x32_bf16 v[48:51], v[152:155], v[80:83], v[48:51]\n"
      "ds_read_b64_tr_b16 v[136:137], v98 offset:53248\n"
      "ds_read_b64_tr_b16 v[138:139], v98 offset:54272\n"
      "v_mfma_f32_16x16x32_bf16 v[40:43], v[144:147], v[84:87], v[40:43]\n"
      "ds_read_b128 v[76:79], v92 offset:59392\n"
      "v_mfma_f32_16x16x32_bf16 v[56:59], v[152:155], v[84:87], v[56:59]\n"
      "ds_read_b64_tr_b16 v[132:133], v99 offset:49152\n"
      "ds_read_b64_tr_b16 v[134:135], v99 offset:50176\n"
      "s_add_u32 s88, s88, 0x8000\n"
      "s_addc_u32 s89, s89, 0\n"
      "global_load_lds_dwordx4 v9, s[88:89]\n"
      "s_add_u32 m0, %[m0b], 114688\n"
      "s_waitcnt lgkmcnt(8)\n"
      "v_mfma_f32_16x16x32_bf16 v[36:39], v[148:151], v[80:83], v[36:39]\n"
      "ds_read_b64_tr_b16 v[140:141], v99 offset:53248\n"
      "ds_read_b64_tr_b16 v[142:143], v99 offset:54272\n"
      "v_mfma_f32_16x16x32_bf16 v[52:55], v[156:159], v[80:83], v[52:55]\n"
      "s_add_u32 s88, s44, s56\n"
      "s_addc_u32 s89, s45, 0\n"
      "s_add_u32 s56, s56, 0x10000\n"
      "s_cmp_eq_u32 s56, 0xe0000\n"
      "s_cselect_b32 s56, 0, s56\n"
      "global_load_lds_dwordx4 v9, s[88:89]\n"
      "s_add_u32 m0, %[m0b], 115712\n"
      "v_mfma_f32_16x16x32_bf16 v[44:47], v[148:151], v[84:87], v[44:47]\n"
      "s_add_u32 s88, s88, 0x8000\n"
      "s_addc_u32 s89, s89, 0\n"
      "global_load_lds_dwordx4 v9, s[88:89]\n"
      "s_add_u32 m0, %[m0b], 106496\n"
      "v_mfma_f32_16x16x32_bf16 v[60:63], v[156:159], v[84:87], v[60:63]\n"
      "s_waitcnt lgkmcnt(0)\n"
      "v_mfma_f32_16x16x32_bf16 v[32:35], v[128:131], v[72:75], v[32:35]\n"
      "v_mfma_f32_16x16x32_bf16 v[36:39], v[132:135], v[72:75], v[36:39]\n"
      "v_mfma_f32_16x16x32_bf16 v[40:43], v[128:131], v[76:79], v[40:43]\n"
      "v_mfma_f32_16x16x32_bf16 v[44:47], v[132:135], v[76:79], v[44:47]\n"
      "v_mfma_f32_16x16x32_bf16 v[48:51], v[136:139], v[72:75], v[48:51]\n"
      "v_mfma_f32_16x16x32_bf16 v[52:55], v[140:143], v[72:75], v[52:55]\n"
      "v_mfma_f32_16x16x32_bf16 v[56:59], v[136:139], v[76:79], v[56:59]\n"
      "v_mfma_f32_16x16x32_bf16 v[60:63], v[140:143], v[76:79], v[60:63]\n"
      "s_waitcnt vmcnt(13) lgkmcnt(0)\n"
      "s_barrier\n"
      "ds_read_b128 v[80:83], v93 offset:57344\n"
      "ds_read_b64_tr_b16 v[144:145], v98 offset:40960\n"
      "ds_read_b64_tr_b16 v[146:147], v98 offset:41984\n"
      "ds_read_b64_tr_b16 v[148:149], v99 offset:40960\n"
      "ds_read_b64_tr_b16 v[150:151], v99 offset:41984\n"
      "ds_read_b64_tr_b16 v[152:153], v98 offset:45056\n"
      "ds_read_b64_tr_b16 v[154:155], v98 offset:46080\n"
      "ds_read_b64_tr_b16 v[156:157], v99 offset:45056\n"
      "ds_read_b64_tr_b16 v[158:159], v99 offset:46080\n"
      "ds_read_b128 v[84:87], v93 offset:59392\n"
      "s_waitcnt lgkmcnt(7)\n"
      "v_mfma_f32_16x16x32_bf16 v[32:35], v[144:147], v[80:83], v[32:35]\n"
      "s_waitcnt lgkmcnt(5)\n"
      "v_mfma_f32_16x16x32_bf16 v[36:39], v[148:151], v[80:83], v[36:39]\n"
      "s_waitcnt lgkmcnt(3)\n"
      "v_mfma_f32_16x16x32_bf16 v[48:51], v[152:155], v[80:83], v[48:51]\n"
      "s_waitcnt lgkmcnt(1)\n"
      "v_mfma_f32_16x16x32_bf16 v[52:55], v[156:159], v[80:83], v[52:55]\n"
      "s_waitcnt lgkmcnt(0)\n"
      "v_mfma_f32_16x16x32_bf16 v[40:43], v[144:147], v[84:87], v[40:43]\n"
      "v_mfma_f32_16x16x32_bf16 v[44:47], v[148:151], v[84:87], v[44:47]\n"
      "v_mfma_f32_16x16x32_bf16 v[56:59], v[152:155], v[84:87], v[56:59]\n"
      "v_mfma_f32_16x16x32_bf16 v[60:63], v[156:159], v[84:87], v[60:63]\n"
      "s_nop 0\n"
      "s_nop 1\n"
      "v_cvt_pk_bf16_f32 v128, v32, v33\n"
      "v_cvt_pk_bf16_f32 v129, v34, v35\n"
      "v_cvt_pk_bf16_f32 v130, v48, v49\n"
      "v_cvt_pk_bf16_f32 v131, v50, v51\n"
      "v_cvt_pk_bf16_f32 v132, v36, v37\n"
      "v_cvt_pk_bf16_f32 v133, v38, v39\n"
      "v_cvt_pk_bf16_f32 v134, v52, v53\n"
      "v_cvt_pk_bf16_f32 v135, v54, v55\n"
      "v_lshlrev_b32 v32, 16, v128\n"
      "v_and_b32 v33, 0xffff0000, v128\n"
      "v_lshlrev_b32 v34, 16, v129\n"
      "v_and_b32 v35, 0xffff0000, v129\n"
      "v_lshlrev_b32 v36, 16, v132\n"
      "v_and_b32 v37, 0xffff0000, v132\n"
      "v_lshlrev_b32 v38, 16, v133\n"
      "v_and_b32 v39, 0xffff0000, v133\n"
      "v_pk_mul_f32 v[136:137], v[32:33], s[62:63]\n"
      "v_pk_mul_f32 v[138:139], v[34:35], s[62:63]\n"
      "v_pk_mul_f32 v[140:141], v[36:37], s[62:63]\n"
      "v_pk_mul_f32 v[142:143], v[38:39], s[62:63]\n"
      "v_exp_f32 v136, v136\n"
      "v_exp_f32 v137, v137\n"
      "v_exp_f32 v138, v138\n"
      "v_exp_f32 v139, v139\n"
      "v_exp_f32 v140, v140\n"
      "v_exp_f32 v141, v141\n"
      "v_exp_f32 v142, v142\n"
      "v_exp_f32 v143, v143\n"
      "v_lshlrev_b32 v48, 16, v130\n"
      "v_and_b32 v49, 0xffff0000, v130\n"
      "v_lshlrev_b32 v50, 16, v131\n"
      "v_and_b32 v51, 0xffff0000, v131\n"
      "v_lshlrev_b32 v52, 16, v134\n"
      "v_and_b32 v53, 0xffff0000, v134\n"
      "v_lshlrev_b32 v54, 16, v135\n"
      "v_and_b32 v55, 0xffff0000, v135\n"
      "v_pk_add_f32 v[136:137], v[136:137], 1.0 op_sel_hi:[1,0]\n"
      "v_pk_add_f32 v[138:139], v[138:139], 1.0 op_sel_hi:[1,0]\n"
      "v_pk_add_f32 v[140:141], v[140:141], 1.0 op_sel_hi:[1,0]\n"
      "v_pk_add_f32 v[142:143], v[142:143], 1.0 op_sel_hi:[1,0]\n"
      "v_rcp_f32 v136, v136\n"
      "v_rcp_f32 v137, v137\n"
      "v_rcp_f32 v138, v138\n"
      "v_rcp_f32 v139, v139\n"
      "v_rcp_f32 v140, v140\n"
      "v_rcp_f32 v141, v141\n"
      "v_rcp_f32 v142, v142\n"
      "v_rcp_f32 v143, v143\n"
      "s_nop 0\n"
      "v_pk_mul_f32 v[32:33], v[32:33], v[136:137]\n"
      "v_pk_mul_f32 v[34:35], v[34:35], v[138:139]\n"
      "v_pk_mul_f32 v[36:37], v[36:37], v[140:141]\n"
      "v_pk_mul_f32 v[38:39], v[38:39], v[142:143]\n"
      "v_cvt_pk_bf16_f32 v128, v32, v33\n"
      "v_cvt_pk_bf16_f32 v129, v34, v35\n"
      "v_cvt_pk_bf16_f32 v130, v36, v37\n"
      "v_cvt_pk_bf16_f32 v131, v38, v39\n"
      "v_lshlrev_b32 v32, 16, v128\n"
      "v_and_b32 v33, 0xffff0000, v128\n"
      "v_lshlrev_b32 v34, 16, v129\n"
      "v_and_b32 v35, 0xffff0000, v129\n"
      "v_lshlrev_b32 v36, 16, v130\n"
      "v_and_b32 v37, 0xffff0000, v130\n"
      "v_lshlrev_b32 v38, 16, v131\n"
      "v_and_b32 v39, 0xffff0000, v131\n"
      "v_pk_mul_f32 v[32:33], v[32:33], v[48:49]\n"
      "v_pk_mul_f32 v[34:35], v[34:35], v[50:51]\n"
      "v_pk_mul_f32 v[36:37], v[36:37], v[52:53]\n"
      "v_pk_mul_f32 v[38:39], v[38:39], v[54:55]\n"
      "v_cvt_pk_bf16_f32 v144, v32, v33\n"
      "v_cvt_pk_bf16_f32 v145, v34, v35\n"
      "v_cvt_pk_bf16_f32 v146, v36, v37\n"
      "v_cvt_pk_bf16_f32 v147, v38, v39\n"
      "v_min_u32 v144, -2, v144\n"
      "v_min_u32 v145, -2, v145\n"
      "v_min_u32 v146, -2, v146\n"
      "v_min_u32 v147, -2, v147\n"
      "s_waitcnt vmcnt(12)\n"
      ".Lfm_%=:\n"
      "v_lshrrev_b32 v8, 16, v7\n"
      "v_cmp_eq_u32 s[84:85], s94, v6\n"
      "v_cmp_eq_u32 s[86:87], s95, v8\n"
      "s_nop 3\n"
      "s_and_b32 s84, s84, s86\n"
      "s_and_b32 s84, s84, 0x1\n"
      "s_cmp_eq_u32 s84, 0x1\n"
      "s_cbranch_scc1 .Lfmok_%=\n"
      "s_sleep 1\n"
      "global_load_dwordx2 v[6:7], v5, %[ws] sc1\n"
      "s_waitcnt vmcnt(0)\n"
      "s_add_u32 s96, s96, 1\n"
      "s_cmp_lt_u32 s96, 4194304\n"
      "s_cbranch_scc1 .Lfm_%=\n"
      ".Lfmok_%=:\n"
      "global_store_dwordx4 v4, v[144:147], s[42:43] sc1\n"
      "ds_write_b128 v106, v[144:147] offset:0\n"
      "v_cvt_pk_bf16_f32 v128, v40, v41\n"
      "v_cvt_pk_bf16_f32 v129, v42, v43\n"
      "v_cvt_pk_bf16_f32 v130, v56, v57\n"
      "v_cvt_pk_bf16_f32 v131, v58, v59\n"
      "v_cvt_pk_bf16_f32 v132, v44, v45\n"
      "v_cvt_pk_bf16_f32 v133, v46, v47\n"
      "v_cvt_pk_bf16_f32 v134, v60, v61\n"
      "v_cvt_pk_bf16_f32 v135, v62, v63\n"
      "v_lshlrev_b32 v40, 16, v128\n"
      "v_and_b32 v41, 0xffff0000, v128\n"
      "v_lshlrev_b32 v42, 16, v129\n"
      "v_and_b32 v43, 0xffff0000, v129\n"
      "v_lshlrev_b32 v44, 16, v132\n"
      "v_and_b32 v45, 0xffff0000, v132\n"
      "v_lshlrev_b32 v46, 16, v133\n"
      "v_and_b32 v47, 0xffff0000, v133\n"
      "v_pk_mul_f32 v[136:137], v[40:41], s[62:63]\n"
      "v_pk_mul_f32 v[138:139], v[42:43], s[62:63]\n"
      "v_pk_mul_f32 v[140:141], v[44:45], s[62:63]\n"
      "v_pk_mul_f32 v[142:143], v[46:47], s[62:63]\n"
      "v_exp_f32 v136, v136\n"
      "v_exp_f32 v137, v137\n"
      "v_exp_f32 v138, v138\n"
      "v_exp_f32 v139, v139\n"
      "v_exp_f32 v140, v140\n"
      "v_exp_f32 v141, v141\n"
      "v_exp_f32 v142, v142\n"
      "v_exp_f32 v143, v143\n"
      "v_lshlrev_b32 v56, 16, v130\n"
      "v_and_b32 v57, 0xffff0000, v130\n"
      "v_lshlrev_b32 v58, 16, v131\n"
      "v_and_b32 v59, 0xffff0000, v131\n"
      "v_lshlrev_b32 v60, 16, v134\n"
      "v_and_b32 v61, 0xffff0000, v134\n"
      "v_lshlrev_b32 v62, 16, v135\n"
      "v_and_b32 v63, 0xffff0000, v135\n"
      "v_pk_add_f32 v[136:137], v[136:137], 1.0 op_sel_hi:[1,0]\n"
      "v_pk_add_f32 v[138:139], v[138:139], 1.0 op_sel_hi:[1,0]\n"
      "v_pk_add_f32 v[140:141], v[140:141], 1.0 op_sel_hi:[1,0]\n"
      "v_pk_add_f32 v[142:143], v[142:143], 1.0 op_sel_hi:[1,0]\n"
      "v_rcp_f32 v136, v136\n"
      "v_rcp_f32 v137, v137\n"
      "v_rcp_f32 v138, v138\n"
      "v_rcp_f32 v139, v139\n"
      "v_rcp_f32 v140, v140\n"
      "v_rcp_f32 v141, v141\n"
      "v_rcp_f32 v142, v142\n"
      "v_rcp_f32 v143, v143\n"
      "s_nop 0\n"
      "v_pk_mul_f32 v[40:41], v[40:41], v[136:137]\n"
      "v_pk_mul_f32 v[42:43], v[42:43], v[138:139]\n"
      "v_pk_mul_f32 v[44:45], v[44:45], v[140:141]\n"
      "v_pk_mul_f32 v[46:47], v[46:47], v[142:143]\n"
      "v_cvt_pk_bf16_f32 v128, v40, v41\n"
      "v_cvt_pk_bf16_f32 v129, v42, v43\n"
      "v_cvt_pk_bf16_f32 v130, v44, v45\n"
      "v_cvt_pk_bf16_f32 v131, v46, v47\n"
      "v_lshlrev_b32 v40, 16, v128\n"
      "v_and_b32 v41, 0xffff0000, v128\n"
      "v_lshlrev_b32 v42, 16, v129\n"
      "v_and_b32 v43, 0xffff0000, v129\n"
      "v_lshlrev_b32 v44, 16, v130\n"
      "v_and_b32 v45, 0xffff0000, v130\n"
      "v_lshlrev_b32 v46, 16, v131\n"
      "v_and_b32 v47, 0xffff0000, v131\n"
      "v_pk_mul_f32 v[40:41], v[40:41], v[56:57]\n"
      "v_pk_mul_f32 v[42:43], v[42:43], v[58:59]\n"
      "v_pk_mul_f32 v[44:45], v[44:45], v[60:61]\n"
      "v_pk_mul_f32 v[46:47], v[46:47], v[62:63]\n"
      "v_cvt_pk_bf16_f32 v148, v40, v41\n"
      "v_cvt_pk_bf16_f32 v149, v42, v43\n"
      "v_cvt_pk_bf16_f32 v150, v44, v45\n"
      "v_cvt_pk_bf16_f32 v151, v46, v47\n"
      "v_min_u32 v148, -2, v148\n"
      "v_min_u32 v149, -2, v149\n"
      "v_min_u32 v150, -2, v150\n"
      "v_min_u32 v151, -2, v151\n"
      "global_store_dwordx4 v105, v[148:151], s[42:43] sc1\n"
      "ds_write_b128 v106, v[148:151] offset:2048\n"
      "s_mov_b32 s90, 0\n"
      "s_waitcnt vmcnt(10) lgkmcnt(0)\n"
      "s_barrier\n"
      "ds_read_b128 v[64:67], v107 offset:0\n"
      "ds_read_b128 v[68:71], v107 offset:1024\n"
      "s_add_u32 s88, s44, s56\n"
      "s_addc_u32 s89, s45, 0\n"
      "s_add_u32 s56, s56, 0x10000\n"
      "s_cmp_eq_u32 s56, 0xe0000\n"
      "s_cselect_b32 s56, 0, s56\n"
      "global_load_lds_dwordx4 v9, s[88:89]\n"
      "s_add_u32 m0, %[m0b], 107520\n"
      "s_add_u32 s88, s88, 0x8000\n"
      "s_addc_u32 s89, s89, 0\n"
      "global_load_lds_dwordx4 v9, s[88:89]\n"
      "s_add_u32 m0, %[m0b], 98304\n"
      "ds_read_b64_tr_b16 v[192:193], v20 offset:24576\n"
      "ds_read_b64_tr_b16 v[194:195], v20 offset:25600\n"
      "ds_read_b64_tr_b16 v[196:197], v21 offset:24576\n"
      "ds_read_b64_tr_b16 v[198:199], v21 offset:25600\n"
      "ds_read_b64_tr_b16 v[200:201], v22 offset:24576\n"
      "ds_read_b64_tr_b16 v[202:203], v22 offset:25600\n"
      "ds_read_b64_tr_b16 v[204:205], v23 offset:24576\n"
      "ds_read_b64_tr_b16 v[206:207], v23 offset:25600\n"
      "ds_read_b64_tr_b16 v[208:209], v20 offset:28672\n"
      "ds_read_b64_tr_b16 v[210:211], v20 offset:29696\n"
      "ds_read_b64_tr_b16 v[212:213], v21 offset:28672\n"
      "ds_read_b64_tr_b16 v[214:215], v21 offset:29696\n"
      "ds_read_b64_tr_b16 v[216:217], v22 offset:28672\n"
      "ds_read_b64_tr_b16 v[218:219], v22 offset:29696\n"
      "ds_read_b64_tr_b16 v[220:221], v23 offset:28672\n"
      "ds_read_b64_tr_b16 v[222:223], v23 offset:29696\n"
      "s_waitcnt lgkmcnt(14)\n"
      "v_mfma_f32_16x16x32_bf16 v[32:35], v[192:195], v[64:67], 0\n"
      "ds_read_b64_tr_b16 v[224:225], v20 offset:16384\n"
      "ds_read_b64_tr_b16 v[226:227], v20 offset:17408\n"
      "s_waitcnt lgkmcnt(14)\n"
      "v_mfma_f32_16x16x32_bf16 v[36:39], v[196:199], v[64:67], 0\n"
      "ds_read_b64_tr_b16 v[228:229], v21 offset:16384\n"
      "ds_read_b64_tr_b16 v[230:231], v21 offset:17408\n"
      "s_waitcnt lgkmcnt(14)\n"
      "v_mfma_f32_16x16x32_bf16 v[40:43], v[200:203], v[64:67], 0\n"
      "ds_read_b64_tr_b16 v[232:233], v22 offset:16384\n"
      "ds_read_b64_tr_b16 v[234:235], v22 offset:17408\n"
      "s_waitcnt lgkmcnt(14)\n"
      "v_mfma_f32_16x16x32_bf16 v[44:47], v[204:207], v[64:67], 0\n"
      "ds_read_b64_tr_b16 v[236:237], v23 offset:16384\n"
      "ds_read_b64_tr_b16 v[238:239], v23 offset:17408\n"
      "s_waitcnt lgkmcnt(14)\n"
      "v_mfma_f32_16x16x32_bf16 v[48:51], v[208:211], v[64:67], 0\n"
      "ds_read_b64_tr_b16 v[240:241], v20 offset:20480\n"
      "ds_read_b64_tr_b16 v[242:243], v20 offset:21504\n"
      "s_waitcnt lgkmcnt(14)\n"
      "v_mfma_f32_16x16x32_bf16 v[52:55], v[212:215], v[64:67], 0\n"
      "ds_read_b64_tr_b16 v[244:245], v21 offset:20480\n"
      "ds_read_b64_tr_b16 v[246:247], v21 offset:21504\n"
      "s_waitcnt lgkmcnt(14)\n"
      "v_mfma_f32_16x16x32_bf16 v[56:59], v[216:219], v[64:67], 0\n"
      "ds_read_b64_tr_b16 v[248:249], v22 offset:20480\n"
      "ds_read_b64_tr_b16 v[250:251], v22 offset:21504\n"
      "s_waitcnt lgkmcnt(14)\n"
      "v_mfma_f32_16x16x32_bf16 v[60:63], v[220:223], v[64:67], 0\n"
      "ds_read_b64_tr_b16 v[252:253], v23 offset:20480\n"
      "ds_read_b64_tr_b16 v[254:255], v23 offset:21504\n"
      "s_waitcnt lgkmcnt(14)\n"
      "v_mfma_f32_16x16x32_bf16 v[32:35], v[224:227], v[68:71], v[32:35]\n"
      "s_waitcnt lgkmcnt(12)\n"
      "v_mfma_f32_16x16x32_bf16 v[36:39], v[228:231], v[68:71], v[36:39]\n"
      "s_waitcnt lgkmcnt(10)\n"
      "v_mfma_f32_16x16x32_bf16 v[40:43], v[232:235], v[68:71], v[40:43]\n"
      "s_waitcnt lgkmcnt(8)\n"
      "v_mfma_f32_16x16x32_bf16 v[44:47], v[236:239], v[68:71], v[44:47]\n"
      "s_waitcnt lgkmcnt(6)\n"
      "v_mfma_f32_16x16x32_bf16 v[48:51], v[240:243], v[68:71], v[48:51]\n"
      "s_waitcnt lgkmcnt(4)\n"
      "v_mfma_f32_16x16x32_bf16 v[52:55], v[244:247], v[68:71], v[52:55]\n"
      "s_waitcnt lgkmcnt(2)\n"
      "v_mfma_f32_16x16x32_bf16 v[56:59], v[248:251], v[68:71], v[56:59]\n"
      "s_waitcnt lgkmcnt(0)\n"
      "v_mfma_f32_16x16x32_bf16 v[60:63], v[252:255], v[68:71], v[60:63]\n"
      "s_waitcnt vmcnt(8)\n"
      "s_barrier\n"
      "s_add_u32 s88, s44, s56\n"
      "s_addc_u32 s89, s45, 0\n"
      "s_add_u32 s56, s56, 0x10000\n"
      "s_cmp_eq_u32 s56, 0xe0000\n"
      "s_cselect_b32 s56, 0, s56\n"
      "global_load_lds_dwordx4 v9, s[88:89]\n"
      "s_add_u32 m0, %[m0b], 99328\n"
      "s_add_u32 s88, s88, 0x8000\n"
      "s_addc_u32 s89, s89, 0\n"
      "global_load_lds_dwordx4 v9, s[88:89]\n"
      "s_add_u32 m0, %[m0b], 90112\n"
      "s_add_u32 s88, s44, s56\n"
      "s_addc_u32 s89, s45, 0\n"
      "s_add_u32 s56, s56, 0x10000\n"
      "s_cmp_eq_u32 s56, 0xe0000\n"
      "s_cselect_b32 s56, 0, s56\n"
      "global_load_lds_dwordx4 v9, s[88:89]\n"
      "s_add_u32 m0, %[m0b], 91136\n"
      "s_add_u32 s88, s88, 0x8000\n"
      "s_addc_u32 s89, s89, 0\n"
      "global_load_lds_dwordx4 v9, s[88:89]\n"
      "s_add_u32 m0, %[m0b], 81920\n"
      "ds_read_b64_tr_b16 a[0:1], v20 offset:8192\n"
      "ds_read_b64_tr_b16 a[2:3], v20 offset:9216\n"
      "ds_read_b64_tr_b16 a[4:5], v21 offset:8192\n"
      "ds_read_b64_tr_b16 a[6:7], v21 offset:9216\n"
      "ds_read_b64_tr_b16 a[8:9], v22 offset:8192\n"
      "ds_read_b64_tr_b16 a[10:11], v22 offset:9216\n"
      "ds_read_b64_tr_b16 a[12:13], v23 offset:8192\n"
      "ds_read_b64_tr_b16 a[14:15], v23 offset:9216\n"
      "ds_read_b64_tr_b16 a[16:17], v20 offset:12288\n"
      "ds_read_b64_tr_b16 a[18:19], v20 offset:13312\n"
      "ds_read_b64_tr_b16 a[20:21], v21 offset:12288\n"
      "ds_read_b64_tr_b16 a[22:23], v21 offset:13312\n"
      "ds_read_b64_tr_b16 a[24:25], v22 offset:12288\n"
      "ds_read_b64_tr_b16 a[26:27], v22 offset:13312\n"
      "ds_read_b64_tr_b16 a[28:29], v23 offset:12288\n"
      "ds_read_b64_tr_b16 a[30:31], v23 offset:13312\n"
      "ds_read_b64_tr_b16 a[32:33], v20 offset:0\n"
      "ds_read_b64_tr_b16 a[34:35], v20 offset:1024\n"
      "ds_read_b64_tr_b16 a[36:37], v21 offset:0\n"
      "ds_read_b64_tr_b16 a[38:39], v21 offset:1024\n"
      "ds_read_b64_tr_b16 a[40:41], v22 offset:0\n"
      "ds_read_b64_tr_b16 a[42:43], v22 offset:1024\n"
      "ds_read_b64_tr_b16 a[44:45], v23 offset:0\n"
      "ds_read_b64_tr_b16 a[46:47], v23 offset:1024\n"
      "ds_read_b64_tr_b16 a[48:49], v20 offset:4096\n"
      "ds_read_b64_tr_b16 a[50:51], v20 offset:5120\n"
      "ds_read_b64_tr_b16 a[52:53], v21 offset:4096\n"
      "ds_read_b64_tr_b16 a[54:55], v21 offset:5120\n"
      "ds_read_b64_tr_b16 a[56:57], v22 offset:4096\n"
      "ds_read_b64_tr_b16 a[58:59], v22 offset:5120\n"
      "ds_read_b64_tr_b16 a[60:61], v23 offset:4096\n"
      "ds_read_b64_tr_b16 a[62:63], v23 offset:5120\n"
      "s_waitcnt vmcnt(8)\n"
      "s_barrier\n"
      "s_add_u32 s88, s44, s56\n"
      "s_addc_u32 s89, s45, 0\n"
      "s_add_u32 s56, s56, 0x10000\n"
      "s_cmp_eq_u32 s56, 0xe0000\n"
      "s_cselect_b32 s56, 0, s56\n"
      "global_load_lds_dwordx4 v9, s[88:89]\n"
      "s_add_u32 m0, %[m0b], 82944\n"
      "s_add_u32 s88, s88, 0x8000\n"
      "s_addc_u32 s89, s89, 0\n"
      "global_load_lds_dwordx4 v9, s[88:89]\n"
      "s_add_u32 m0, %[m0b], 73728\n"
      "global_load_dwordx4 v[72:75], v4, s[70:71] offset:0 sc1\n"
      "global_load_dwordx4 v[76:79], v4, s[70:71] offset:1024 sc1\n"
      "ds_read_b64_tr_b16 a[64:65], v16 offset:57344\n"
      "ds_read_b64_tr_b16 a[66:67], v16 offset:58368\n"
      "ds_read_b64_tr_b16 a[68:69], v17 offset:57344\n"
      "ds_read_b64_tr_b16 a[70:71], v17 offset:58368\n"
      "ds_read_b64_tr_b16 a[72:73], v18 offset:57344\n"
      "ds_read_b64_tr_b16 a[74:75], v18 offset:58368\n"
      "ds_read_b64_tr_b16 a[76:77], v19 offset:57344\n"
      "ds_read_b64_tr_b16 a[78:79], v19 offset:58368\n"
      "ds_read_b64_tr_b16 a[80:81], v16 offset:61440\n"
      "ds_read_b64_tr_b16 a[82:83], v16 offset:62464\n"
      "ds_read_b64_tr_b16 a[84:85], v17 offset:61440\n"
      "ds_read_b64_tr_b16 a[86:87], v17 offset:62464\n"
      "ds_read_b64_tr_b16 a[88:89], v18 offset:61440\n"
      "ds_read_b64_tr_b16 a[90:91], v18 offset:62464\n"
      "ds_read_b64_tr_b16 a[92:93], v19 offset:61440\n"
      "ds_read_b64_tr_b16 a[94:95], v19 offset:62464\n"
      "ds_read_b64_tr_b16 a[96:97], v16 offset:49152\n"
      "ds_read_b64_tr_b16 a[98:99], v16 offset:50176\n"
      "ds_read_b64_tr_b16 a[100:101], v17 offset:49152\n"
      "ds_read_b64_tr_b16 a[102:103], v17 offset:50176\n"
      "ds_read_b64_tr_b16 a[104:105], v18 offset:49152\n"
      "ds_read_b64_tr_b16 a[106:107], v18 offset:50176\n"
      "ds_read_b64_tr_b16 a[108:109], v19 offset:49152\n"
      "ds_read_b64_tr_b16 a[110:111], v19 offset:50176\n"
      "ds_read_b64_tr_b16 a[112:113], v16 offset:53248\n"
      "ds_read_b64_tr_b16 a[114:115], v16 offset:54272\n"
      "ds_read_b64_tr_b16 a[116:117], v17 offset:53248\n"
      "ds_read_b64_tr_b16 a[118:119], v17 offset:54272\n"
      "ds_read_b64_tr_b16 a[120:121], v18 offset:53248\n"
      "ds_read_b64_tr_b16 a[122:123], v18 offset:54272\n"
      "ds_read_b64_tr_b16 a[124:125], v19 offset:53248\n"
      "ds_read_b64_tr_b16 a[126:127], v19 offset:54272\n"
      "s_waitcnt vmcnt(6)\n"
      "s_barrier\n"
      "s_add_u32 s88, s44, s56\n"
      "s_addc_u32 s89, s45, 0\n"
      "s_add_u32 s56, s56, 0x10000\n"
      "s_cmp_eq_u32 s56, 0xe0000\n"
      "s_cselect_b32 s56, 0, s56\n"
      "global_load_lds_dwordx4 v9, s[88:89]\n"
      "s_add_u32 m0, %[m0b], 74752\n"
      "s_add_u32 s88, s88, 0x8000\n"
      "s_addc_u32 s89, s89, 0\n"
      "global_load_lds_dwordx4 v9, s[88:89]\n"
      "s_add_u32 m0, %[m0b], 65536\n"
      "s_add_u32 s88, s44, s56\n"
      "s_addc_u32 s89, s45, 0\n"
      "s_add_u32 s56, s56, 0x10000\n"
      "s_cmp_eq_u32 s56, 0xe0000\n"
      "s_cselect_b32 s56, 0, s56\n"
      "global_load_lds_dwordx4 v9, s[88:89]\n"
      "s_add_u32 m0, %[m0b], 66560\n"
      "s_add_u32 s88, s88, 0x8000\n"
      "s_addc_u32 s89, s89, 0\n"
      "global_load_lds_dwordx4 v9, s[88:89]\n"
      "s_add_u32 m0, %[m0b], 57344\n"
      "global_load_dwordx4 v[80:83], v4, s[72:73] offset:0 sc1\n"
      "global_load_dwordx4 v[84:87], v4, s[72:73] offset:1024 sc1\n"
      "ds_read_b64_tr_b16 a[128:129], v16 offset:40960\n"
      "ds_read_b64_tr_b16 a[130:131], v16 offset:41984\n"
      "ds_read_b64_tr_b16 a[132:133], v17 offset:40960\n"
      "ds_read_b64_tr_b16 a[134:135], v17 offset:41984\n"
      "ds_read_b64_tr_b16 a[136:137], v18 offset:40960\n"
      "ds_read_b64_tr_b16 a[138:139], v18 offset:41984\n"
      "ds_read_b64_tr_b16 a[140:141], v19 offset:40960\n"
      "ds_read_b64_tr_b16 a[142:143], v19 offset:41984\n"
      "ds_read_b64_tr_b16 a[144:145], v16 offset:45056\n"
      "ds_read_b64_tr_b16 a[146:147], v16 offset:46080\n"
      "ds_read_b64_tr_b16 a[148:149], v17 offset:45056\n"
      "ds_read_b64_tr_b16 a[150:151], v17 offset:46080\n"
      "ds_read_b64_tr_b16 a[152:153], v18 offset:45056\n"
      "ds_read_b64_tr_b16 a[154:155], v18 offset:46080\n"
      "ds_read_b64_tr_b16 a[156:157], v19 offset:45056\n"
      "ds_read_b64_tr_b16 a[158:159], v19 offset:46080\n"
      "ds_read_b64_tr_b16 a[160:161], v16 offset:32768\n"
      "ds_read_b64_tr_b16 a[162:163], v16 offset:33792\n"
      "ds_read_b64_tr_b16 a[164:165], v17 offset:32768\n"
      "ds_read_b64_tr_b16 a[166:167], v17 offset:33792\n"
      "ds_read_b64_tr_b16 a[168:169], v18 offset:32768\n"
      "ds_read_b64_tr_b16 a[170:171], v18 offset:33792\n"
      "ds_read_b64_tr_b16 a[172:173], v19 offset:32768\n"
      "ds_read_b64_tr_b16 a[174:175], v19 offset:33792\n"
      "ds_read_b64_tr_b16 a[176:177], v16 offset:36864\n"
      "ds_read_b64_tr_b16 a[178:179], v16 offset:37888\n"
      "global_load_dwordx4 v[88:91], v4, s[74:75] offset:0 sc1\n"
      "global_load_dwordx4 v[92:95], v4, s[74:75] offset:1024 sc1\n"
      "s_add_u32 s88, s44, s56\n"
      "s_addc_u32 s89, s45, 0\n"
      "s_add_u32 s56, s56, 0x10000\n"
      "s_cmp_eq_u32 s56, 0xe0000\n"
      "s_cselect_b32 s56, 0, s56\n"
      "global_load_lds_dwordx4 v9, s[88:89]\n"
      "s_add_u32 m0, %[m0b], 58368\n"
      "s_add_u32 s88, s88, 0x8000\n"
      "s_addc_u32 s89, s89, 0\n"
      "global_load_lds_dwordx4 v9, s[88:89]\n"
      "s_add_u32 m0, %[m0b], 49152\n"
      "s_add_u32 s88, s44, s56\n"
      "s_addc_u32 s89, s45, 0\n"
      "s_add_u32 s56, s56, 0x10000\n"
      "s_cmp_eq_u32 s56, 0xe0000\n"
      "s_cselect_b32 s56, 0, s56\n"
      "global_load_lds_dwordx4 v9, s[88:89]\n"
      "s_add_u32 m0, %[m0b], 50176\n"
      "s_waitcnt vmcnt(12)\n"
      "v_max3_u32 v8, v72, v73, v74\n"
      "v_max_u32 v8, v8, v75\n"
      "v_cmp_eq_u32 vcc, -1, v8\n"
      "ds_read_b64_tr_b16 a[180:181], v17 offset:36864\n"
      "ds_read_b64_tr_b16 a[182:183], v17 offset:37888\n"
      "ds_read_b64_tr_b16 a[184:185], v18 offset:36864\n"
      "ds_read_b64_tr_b16 a[186:187], v18 offset:37888\n"
      "ds_read_b64_tr_b16 a[188:189], v19 offset:36864\n"
      "ds_read_b64_tr_b16 a[190:191], v19 offset:37888\n"
      "s_waitcnt vmcnt(13) lgkmcnt(15)\n"
      "s_barrier\n"
      "s_cbranch_vccnz .Lsf2_%=\n"
      "ds_read_b64_tr_b16 a[192:193], v16 offset:24576\n"
      "ds_read_b64_tr_b16 a[194:195], v16 offset:25600\n"
      "v_mfma_f32_16x16x32_bf16 v[32:35], a[0:3], v[72:75], v[32:35]\n"
      "ds_read_b64_tr_b16 a[196:197], v17 offset:24576\n"
      "ds_read_b64_tr_b16 a[198:199], v17 offset:25600\n"
      "v_mfma_f32_16x16x32_bf16 v[36:39], a[4:7], v[72:75], v[36:39]\n"
      "ds_read_b64_tr_b16 a[200:201], v18 offset:24576\n"
      "ds_read_b64_tr_b16 a[202:203], v18 offset:25600\n"
      "v_mfma_f32_16x16x32_bf16 v[40:43], a[8:11], v[72:75], v[40:43]\n"
      "ds_read_b64_tr_b16 a[204:205], v19 offset:24576\n"
      "ds_read_b64_tr_b16 a[206:207], v19 offset:25600\n"
      "v_mfma_f32_16x16x32_bf16 v[44:47], a[12:15], v[72:75], v[44:47]\n"
      "ds_read_b64_tr_b16 a[208:209], v16 offset:28672\n"
      "ds_read_b64_tr_b16 a[210:211], v16 offset:29696\n"
      "v_mfma_f32_16x16x32_bf16 v[48:51], a[16:19], v[72:75], v[48:51]\n"
      "s_add_u32 s88, s88, 0x8000\n"
      "s_addc_u32 s89, s89, 0\n"
      "global_load_lds_dwordx4 v9, s[88:89]\n"
      "s_waitcnt vmcnt(12)\n"
      "v_max3_u32 v8, v76, v77, v78\n"
      "v_max_u32 v8, v8, v79\n"
      "v_cmp_eq_u32 vcc, -1, v8\n"
      "ds_read_b64_tr_b16 a[212:213], v17 offset:28672\n"
      "ds_read_b64_tr_b16 a[214:215], v17 offset:29696\n"
      "v_mfma_f32_16x16x32_bf16 v[52:55], a[20:23], v[72:75], v[52:55]\n"
      "ds_read_b64_tr_b16 a[216:217], v18 offset:28672\n"
      "ds_read_b64_tr_b16 a[218:219], v18 offset:29696\n"
      "v_mfma_f32_16x16x32_bf16 v[56:59], a[24:27], v[72:75], v[56:59]\n"
      "ds_read_b64_tr_b16 a[220:221], v19 offset:28672\n"
      "ds_read_b64_tr_b16 a[222:223], v19 offset:29696\n"
      "v_mfma_f32_16x16x32_bf16 v[60:63], a[28:31], v[72:75], v[60:63]\n"
      ".Lsn2_%=:\n"
      "s_cbranch_vccnz .Lsf3_%=\n"
      "ds_read_b64_tr_b16 a[224:225], v16 offset:16384\n"
      "ds_read_b64_tr_b16 a[226:227], v16 offset:17408\n"
      "v_mfma_f32_16x16x32_bf16 v[32:35], a[32:35], v[76:79], v[32:35]\n"
      "ds_read_b64_tr_b16 a[228:229], v17 offset:16384\n"
      "ds_read_b64_tr_b16 a[230:231], v17 offset:17408\n"
      "v_mfma_f32_16x16x32_bf16 v[36:39], a[36:39], v[76:79], v[36:39]\n"
      "ds_read_b64_tr_b16 a[232:233], v18 offset:16384\n"
      "ds_read_b64_tr_b16 a[234:235], v18 offset:17408\n"
      "v_mfma_f32_16x16x32_bf16 v[40:43], a[40:43], v[76:79], v[40:43]\n"
      "ds_read_b64_tr_b16 a[236:237], v19 offset:16384\n"
      "ds_read_b64_tr_b16 a[238:239], v19 offset:17408\n"
      "v_mfma_f32_16x16x32_bf16 v[44:47], a[44:47], v[76:79], v[44:47]\n"
      "ds_read_b64_tr_b16 a[240:241], v16 offset:20480\n"
      "ds_read_b64_tr_b16 a[242:243], v16 offset:21504\n"
      "v_mfma_f32_16x16x32_bf16 v[48:51], a[48:51], v[76:79], v[48:51]\n"
      "global_load_dwordx4 v[96:99], v4, s[76:77] offset:0 sc1\n"
      "global_load_dwordx4 v[100:103], v4, s[76:77] offset:1024 sc1\n"
      "global_load_dwordx4 v[104:107], v4, s[78:79] offset:0 sc1\n"
      "global_load_dwordx4 v[108:111], v4, s[78:79] offset:1024 sc1\n"
      "global_load_dwordx2 v[6:7], v5, %[ws] sc1\n"
      "s_waitcnt vmcnt(12)\n"
      "v_max3_u32 v8, v80, v81, v82\n"
      "v_max_u32 v8, v8, v83\n"
      "v_cmp_eq_u32 vcc, -1, v8\n"
      "ds_read_b64_tr_b16 a[244:245], v17 offset:20480\n"
      "ds_read_b64_tr_b16 a[246:247], v17 offset:21504\n"
      "v_mfma_f32_16x16x32_bf16 v[52:55], a[52:55], v[76:79], v[52:55]\n"
      "ds_read_b64_tr_b16 a[248:249], v18 offset:20480\n"
      "ds_read_b64_tr_b16 a[250:251], v18 offset:21504\n"
      "v_mfma_f32_16x16x32_bf16 v[56:59], a[56:59], v[76:79], v[56:59]\n"
      "ds_read_b64_tr_b16 a[252:253], v19 offset:20480\n"
      "ds_read_b64_tr_b16 a[254:255], v19 offset:21504\n"
      "v_mfma_f32_16x16x32_bf16 v[60:63], a[60:63], v[76:79], v[60:63]\n"
      ".Lsn3_%=:\n"
      "s_waitcnt vmcnt(13)\n"
      "s_barrier\n"
      "s_cbranch_vccnz .Lsf4_%=\n"
      "ds_read_b64_tr_b16 v[128:129], v16 offset:8192\n"
      "ds_read_b64_tr_b16 v[130:131], v16 offset:9216\n"
      "v_mfma_f32_16x16x32_bf16 v[32:35], a[64:67], v[80:83], v[32:35]\n"
      "ds_read_b64_tr_b16 v[132:133], v17 offset:8192\n"
      "ds_read_b64_tr_b16 v[134:135], v17 offset:9216\n"
      "v_mfma_f32_16x16x32_bf16 v[36:39], a[68:71], v[80:83], v[36:39]\n"
      "ds_read_b64_tr_b16 v[136:137], v18 offset:8192\n"
      "ds_read_b64_tr_b16 v[138:139], v18 offset:9216\n"
      "v_mfma_f32_16x16x32_bf16 v[40:43], a[72:75], v[80:83], v[40:43]\n"
      "ds_read_b64_tr_b16 v[140:141], v19 offset:8192\n"
      "ds_read_b64_tr_b16 v[142:143], v19 offset:9216\n"
      "v_mfma_f32_16x16x32_bf16 v[44:47], a[76:79], v[80:83], v[44:47]\n"
      "ds_read_b64_tr_b16 v[144:145], v16 offset:12288\n"
      "ds_read_b64_tr_b16 v[146:147], v16 offset:13312\n"
      "v_mfma_f32_16x16x32_bf16 v[48:51], a[80:83], v[80:83], v[48:51]\n"
      "global_load_dwordx4 v[112:115], v4, s[80:81] offset:0 sc1\n"
      "s_waitcnt vmcnt(12)\n"
      "v_max3_u32 v8, v84, v85, v86\n"
      "v_max_u32 v8, v8, v87\n"
      "v_cmp_eq_u32 vcc, -1, v8\n"
      "ds_read_b64_tr_b16 v[148:149], v17 offset:12288\n"
      "ds_read_b64_tr_b16 v[150:151], v17 offset:13312\n"
      "v_mfma_f32_16x16x32_bf16 v[52:55], a[84:87], v[80:83], v[52:55]\n"
      "ds_read_b64_tr_b16 v[152:153], v18 offset:12288\n"
      "ds_read_b64_tr_b16 v[154:155], v18 offset:13312\n"
      "v_mfma_f32_16x16x32_bf16 v[56:59], a[88:91], v[80:83], v[56:59]\n"
      "ds_read_b64_tr_b16 v[156:157], v19 offset:12288\n"
      "ds_read_b64_tr_b16 v[158:159], v19 offset:13312\n"
      "v_mfma_f32_16x16x32_bf16 v[60:63], a[92:95], v[80:83], v[60:63]\n"
      ".Lsn4_%=:\n"
      "s_cbranch_vccnz .Lsf5_%=\n"
      "ds_read_b64_tr_b16 v[160:161], v16 offset:0\n"
      "ds_read_b64_tr_b16 v[162:163], v16 offset:1024\n"
      "v_mfma_f32_16x16x32_bf16 v[32:35], a[96:99], v[84:87], v[32:35]\n"
      "ds_read_b64_tr_b16 v[164:165], v17 offset:0\n"
      "ds_read_b64_tr_b16 v[166:167], v17 offset:1024\n"
      "v_mfma_f32_16x16x32_bf16 v[36:39], a[100:103], v[84:87], v[36:39]\n"
      "ds_read_b64_tr_b16 v[168:169], v18 offset:0\n"
      "ds_read_b64_tr_b16 v[170:171], v18 offset:1024\n"
      "v_mfma_f32_16x16x32_bf16 v[40:43], a[104:107], v[84:87], v[40:43]\n"
      "ds_read_b64_tr_b16 v[172:173], v19 offset:0\n"
      "ds_read_b64_tr_b16 v[174:175], v19 offset:1024\n"
      "v_mfma_f32_16x16x32_bf16 v[44:47], a[108:111], v[84:87], v[44:47]\n"
      "ds_read_b64_tr_b16 v[176:177], v16 offset:4096\n"
      "ds_read_b64_tr_b16 v[178:179], v16 offset:5120\n"
      "v_mfma_f32_16x16x32_bf16 v[48:51], a[112:115], v[84:87], v[48:51]\n"
      "global_load_dwordx4 v[116:119], v4, s[80:81] offset:1024 sc1\n"
      "s_waitcnt vmcnt(12)\n"
      "v_max3_u32 v8, v88, v89, v90\n"
      "v_max_u32 v8, v8, v91\n"
      "v_cmp_eq_u32 vcc, -1, v8\n"
      "ds_read_b64_tr_b16 v[180:181], v17 offset:4096\n"
      "ds_read_b64_tr_b16 v[182:183], v17 offset:5120\n"
      "v_mfma_f32_16x16x32_bf16 v[52:55], a[116:119], v[84:87], v[52:55]\n"
      "ds_read_b64_tr_b16 v[184:185], v18 offset:4096\n"
      "ds_read_b64_tr_b16 v[186:187], v18 offset:5120\n"
      "v_mfma_f32_16x16x32_bf16 v[56:59], a[120:123], v[84:87], v[56:59]\n"
      "ds_read_b64_tr_b16 v[188:189], v19 offset:4096\n"
      "ds_read_b64_tr_b16 v[190:191], v19 offset:5120\n"
      "v_mfma_f32_16x16x32_bf16 v[60:63], a[124:127], v[84:87], v[60:63]\n"
      ".Lsn5_%=:\n"
      "s_waitcnt vmcnt(7)\n"
      "s_barrier\n"
      "global_store_dwordx4 v4, v[120:123], s[40:41] offset:-4096 sc1\n"
      "global_store_dwordx4 v4, v[120:123], s[40:41] offset:-3072 sc1\n"
      "global_store_dwordx4 v4, v[120:123], s[40:41] offset:-2048 sc1\n"
      "global_store_dwordx4 v4, v[120:123], s[40:41] offset:-1024 sc1\n"
      "s_cbranch_vccnz .Lsf6_%=\n"
      "ds_read_b64_tr_b16 v[192:193], v12 offset:57344\n"
      "ds_read_b64_tr_b16 v[194:195], v12 offset:58368\n"
      "v_mfma_f32_16x16x32_bf16 v[32:35], a[128:131], v[88:91], v[32:35]\n"
      "ds_read_b64_tr_b16 v[196:197], v13 offset:57344\n"
      "ds_read_b64_tr_b16 v[198:199], v13 offset:58368\n"
      "v_mfma_f32_16x16x32_bf16 v[36:39], a[132:135], v[88:91], v[36:39]\n"
      "ds_read_b64_tr_b16 v[200:201], v14 offset:57344\n"
      "ds_read_b64_tr_b16 v[202:203], v14 offset:58368\n"
      "v_mfma_f32_16x16x32_bf16 v[40:43], a[136:139], v[88:91], v[40:43]\n"
      "ds_read_b64_tr_b16 v[204:205], v15 offset:57344\n"
      "ds_read_b64_tr_b16 v[206:207], v15 offset:58368\n"
      "v_mfma_f32_16x16x32_bf16 v[44:47], a[140:143], v[88:91], v[44:47]\n"
      "ds_read_b64_tr_b16 v[208:209], v12 offset:61440\n"
      "ds_read_b64_tr_b16 v[210:211], v12 offset:62464\n"
      "v_mfma_f32_16x16x32_bf16 v[48:51], a[144:147], v[88:91], v[48:51]\n"
      "s_waitcnt vmcnt(15)\n"
      "v_max3_u32 v8, v92, v93, v94\n"
      "v_max_u32 v8, v8, v95\n"
      "v_cmp_eq_u32 vcc, -1, v8\n"
      "ds_read_b64_tr_b16 v[212:213], v13 offset:61440\n"
      "ds_read_b64_tr_b16 v[214:215], v13 offset:62464\n"
      "v_mfma_f32_16x16x32_bf16 v[52:55], a[148:151], v[88:91], v[52:55]\n"
      "ds_read_b64_tr_b16 v[216:217], v14 offset:61440\n"
      "ds_read_b64_tr_b16 v[218:219], v14 offset:62464\n"
      "v_mfma_f32_16x16x32_bf16 v[56:59], a[152:155], v[88:91], v[56:59]\n"
      "ds_read_b64_tr_b16 v[220:221], v15 offset:61440\n"
      "ds_read_b64_tr_b16 v[222:223], v15 offset:62464\n"
      "v_mfma_f32_16x16x32_bf16 v[60:63], a[156:159], v[88:91], v[60:63]\n"
      ".Lsn6_%=:\n"
      "s_waitcnt lgkmcnt(15)\n"
      "s_cbranch_vccnz .Lsf7_%=\n"
      "ds_read_b64_tr_b16 v[224:225], v12 offset:49152\n"
      "ds_read_b64_tr_b16 v[226:227], v12 offset:50176\n"
      "v_mfma_f32_16x16x32_bf16 v[32:35], a[160:163], v[92:95], v[32:35]\n"
      "ds_read_b64_tr_b16 v[228:229], v13 offset:49152\n"
      "ds_read_b64_tr_b16 v[230:231], v13 offset:50176\n"
      "v_mfma_f32_16x16x32_bf16 v[36:39], a[164:167], v[92:95], v[36:39]\n"
      "ds_read_b64_tr_b16 v[232:233], v14 offset:49152\n"
      "ds_read_b64_tr_b16 v[234:235], v14 offset:50176\n"
      "v_mfma_f32_16x16x32_bf16 v[40:43], a[168:171], v[92:95], v[40:43]\n"
      "ds_read_b64_tr_b16 v[236:237], v15 offset:49152\n"
      "ds_read_b64_tr_b16 v[238:239], v15 offset:50176\n"
      "v_mfma_f32_16x16x32_bf16 v[44:47], a[172:175], v[92:95], v[44:47]\n"
      "ds_read_b64_tr_b16 v[240:241], v12 offset:53248\n"
      "ds_read_b64_tr_b16 v[242:243], v12 offset:54272\n"
      "v_mfma_f32_16x16x32_bf16 v[48:51], a[176:179], v[92:95], v[48:51]\n"
      "s_waitcnt vmcnt(10)\n"
      "v_max3_u32 v8, v96, v97, v98\n"
      "v_max_u32 v8, v8, v99\n"
      "v_cmp_eq_u32 vcc, -1, v8\n"
      "ds_read_b64_tr_b16 v[244:245], v13 offset:53248\n"
      "ds_read_b64_tr_b16 v[246:247], v13 offset:54272\n"
      "v_mfma_f32_16x16x32_bf16 v[52:55], a[180:183], v[92:95], v[52:55]\n"
      "ds_read_b64_tr_b16 v[248:249], v14 offset:53248\n"
      "ds_read_b64_tr_b16 v[250:251], v14 offset:54272\n"
      "v_mfma_f32_16x16x32_bf16 v[56:59], a[184:187], v[92:95], v[56:59]\n"
      "ds_read_b64_tr_b16 v[252:253], v15 offset:53248\n"
      "ds_read_b64_tr_b16 v[254:255], v15 offset:54272\n"
      "v_mfma_f32_16x16x32_bf16 v[60:63], a[188:191], v[92:95], v[60:63]\n"
      ".Lsn7_%=:\n"
      "v_lshlrev_b32 v10, 3, %[ln]\n"
      "s_xor_b32 s67, %[xcd], 1\n"
      "s_mul_i32 s67, s67, 32768\n"
      "s_add_u32 s67, s67, 512\n"
      "v_add_u32 v12, s67, v10\n"
      "s_xor_b32 s67, %[xcd], 2\n"
      "s_mul_i32 s67, s67, 32768\n"
      "s_add_u32 s67, s67, 1024\n"
      "v_add_u32 v13, s67, v10\n"
      "s_xor_b32 s67, %[xcd], 3\n"
      "s_mul_i32 s67, s67, 32768\n"
      "s_add_u32 s67, s67, 1536\n"
      "v_add_u32 v14, s67, v10\n"
      "s_xor_b32 s67, %[xcd], 4\n"
      "s_mul_i32 s67, s67, 32768\n"
      "s_add_u32 s67, s67, 2048\n"
      "v_add_u32 v15, s67, v10\n"
      "s_xor_b32 s67, %[xcd], 5\n"
      "s_mul_i32 s67, s67, 32768\n"
      "s_add_u32 s67, s67, 2560\n"
      "v_add_u32 v16, s67, v10\n"
      "s_xor_b32 s67, %[xcd], 6\n"
      "s_mul_i32 s67, s67, 32768\n"
      "s_add_u32 s67, s67, 3072\n"
      "v_add_u32 v17, s67, v10\n"
      "s_xor_b32 s67, %[xcd], 7\n"
      "s_mul_i32 s67, s67, 32768\n"
      "s_add_u32 s67, s67, 3584\n"
      "v_add_u32 v18, s67, v10\n"
      "v_and_b32 v10, 15, %[ln]\n"
      "v_lshrrev_b32 v11, 4, %[ln]\n"
      "v_lshlrev_b32 v11, 3, v11\n"
      "v_lshl_add_u32 v19, v10, 11, v11\n"
      "s_cbranch_vccnz .Lsf8_%=\n"
      "v_mfma_f32_16x16x32_bf16 v[32:35], a[192:195], v[96:99], v[32:35]\n"
      "v_mfma_f32_16x16x32_bf16 v[36:39], a[196:199], v[96:99], v[36:39]\n"
      "v_mfma_f32_16x16x32_bf16 v[40:43], a[200:203], v[96:99], v[40:43]\n"
      "v_mfma_f32_16x16x32_bf16 v[44:47], a[204:207], v[96:99], v[44:47]\n"
      "v_mfma_f32_16x16x32_bf16 v[48:51], a[208:211], v[96:99], v[48:51]\n"
      "s_waitcnt vmcnt(9)\n"
      "v_max3_u32 v8, v100, v101, v102\n"
      "v_max_u32 v8, v8, v103\n"
      "v_cmp_eq_u32 vcc, -1, v8\n"
      "v_mfma_f32_16x16x32_bf16 v[52:55], a[212:215], v[96:99], v[52:55]\n"
      "v_mfma_f32_16x16x32_bf16 v[56:59], a[216:219], v[96:99], v[56:59]\n"
      "v_mfma_f32_16x16x32_bf16 v[60:63], a[220:223], v[96:99], v[60:63]\n"
      ".Lsn8_%=:\n"
      "s_cbranch_vccnz .Lsf9_%=\n"
      "v_mfma_f32_16x16x32_bf16 v[32:35], a[224:227], v[100:103], v[32:35]\n"
      "v_mfma_f32_16x16x32_bf16 v[36:39], a[228:231], v[100:103], v[36:39]\n"
      "v_mfma_f32_16x16x32_bf16 v[40:43], a[232:235], v[100:103], v[40:43]\n"
      "v_mfma_f32_16x16x32_bf16 v[44:47], a[236:239], v[100:103], v[44:47]\n"
      "v_mfma_f32_16x16x32_bf16 v[48:51], a[240:243], v[100:103], v[48:51]\n"
      "s_waitcnt vmcnt(8)\n"
      "v_max3_u32 v8, v104, v105, v106\n"
      "v_max_u32 v8, v8, v107\n"
      "v_cmp_eq_u32 vcc, -1, v8\n"
      "v_mfma_f32_16x16x32_bf16 v[52:55], a[244:247], v[100:103], v[52:55]\n"
      "v_mfma_f32_16x16x32_bf16 v[56:59], a[248:251], v[100:103], v[56:59]\n"
      "v_mfma_f32_16x16x32_bf16 v[60:63], a[252:255], v[100:103], v[60:63]\n"
      ".Lsn9_%=:\n"
      "s_cbranch_vccnz .Lsf10_%=\n"
      "v_mfma_f32_16x16x32_bf16 v[32:35], v[128:131], v[104:107], v[32:35]\n"
      "v_mfma_f32_16x16x32_bf16 v[36:39], v[132:135], v[104:107], v[36:39]\n"
      "v_mfma_f32_16x16x32_bf16 v[40:43], v[136:139], v[104:107], v[40:43]\n"
      "v_mfma_f32_16x16x32_bf16 v[44:47], v[140:143], v[104:107], v[44:47]\n"
      "v_mfma_f32_16x16x32_bf16 v[48:51], v[144:147], v[104:107], v[48:51]\n"
      "s_waitcnt vmcnt(7)\n"
      "v_max3_u32 v8, v108, v109, v110\n"
      "v_max_u32 v8, v8, v111\n"
      "v_cmp_eq_u32 vcc, -1, v8\n"
      "v_mfma_f32_16x16x32_bf16 v[52:55], v[148:151], v[104:107], v[52:55]\n"
      "v_mfma_f32_16x16x32_bf16 v[56:59], v[152:155], v[104:107], v[56:59]\n"
      "v_mfma_f32_16x16x32_bf16 v[60:63], v[156:159], v[104:107], v[60:63]\n"
      ".Lsn10_%=:\n"
      "s_cbranch_vccnz .Lsf11_%=\n"
      "v_mfma_f32_16x16x32_bf16 v[32:35], v[160:163], v[108:111], v[32:35]\n"
      "v_mfma_f32_16x16x32_bf16 v[36:39], v[164:167], v[108:111], v[36:39]\n"
      "v_mfma_f32_16x16x32_bf16 v[40:43], v[168:171], v[108:111], v[40:43]\n"
      "v_mfma_f32_16x16x32_bf16 v[44:47], v[172:175], v[108:111], v[44:47]\n"
      "v_mfma_f32_16x16x32_bf16 v[48:51], v[176:179], v[108:111], v[48:51]\n"
      "s_waitcnt vmcnt(5)\n"
      "v_max3_u32 v8, v112, v113, v114\n"
      "v_max_u32 v8, v8, v115\n"
      "v_cmp_eq_u32 vcc, -1, v8\n"
      "v_mfma_f32_16x16x32_bf16 v[52:55], v[180:183], v[108:111], v[52:55]\n"
      "v_mfma_f32_16x16x32_bf16 v[56:59], v[184:187], v[108:111], v[56:59]\n"
      "v_mfma_f32_16x16x32_bf16 v[60:63], v[188:191], v[108:111], v[60:63]\n"
      ".Lsn11_%=:\n"
      "s_waitcnt lgkmcnt(15)\n"
      "s_cbranch_vccnz .Lsf12_%=\n"
      "v_mfma_f32_16x16x32_bf16 v[32:35], v[192:195], v[112:115], v[32:35]\n"
      "v_mfma_f32_16x16x32_bf16 v[36:39], v[196:199], v[112:115], v[36:39]\n"
      "v_mfma_f32_16x16x32_bf16 v[40:43], v[200:203], v[112:115], v[40:43]\n"
      "v_mfma_f32_16x16x32_bf16 v[44:47], v[204:207], v[112:115], v[44:47]\n"
      "v_mfma_f32_16x16x32_bf16 v[48:51], v[208:211], v[112:115], v[48:51]\n"
      "s_waitcnt vmcnt(4)\n"
      "v_max3_u32 v8, v116, v117, v118\n"
      "v_max_u32 v8, v8, v119\n"
      "v_cmp_eq_u32 vcc, -1, v8\n"
      "v_mfma_f32_16x16x32_bf16 v[52:55], v[212:215], v[112:115], v[52:55]\n"
      "v_mfma_f32_16x16x32_bf16 v[56:59], v[216:219], v[112:115], v[56:59]\n"
      "v_mfma_f32_16x16x32_bf16 v[60:63], v[220:223], v[112:115], v[60:63]\n"
      ".Lsn12_%=:\n"
      "s_waitcnt lgkmcnt(0)\n"
      "s_cbranch_vccnz .Lsf13_%=\n"
      "v_mfma_f32_16x16x32_bf16 v[32:35], v[224:227], v[116:119], v[32:35]\n"
      "v_mfma_f32_16x16x32_bf16 v[36:39], v[228:231], v[116:119], v[36:39]\n"
      "v_mfma_f32_16x16x32_bf16 v[40:43], v[232:235], v[116:119], v[40:43]\n"
      "v_mfma_f32_16x16x32_bf16 v[44:47], v[236:239], v[116:119], v[44:47]\n"
      "v_mfma_f32_16x16x32_bf16 v[48:51], v[240:243], v[116:119], v[48:51]\n"
      "v_mfma_f32_16x16x32_bf16 v[52:55], v[244:247], v[116:119], v[52:55]\n"
      "v_mfma_f32_16x16x32_bf16 v[56:59], v[248:251], v[116:119], v[56:59]\n"
      "v_mfma_f32_16x16x32_bf16 v[60:63], v[252:255], v[116:119], v[60:63]\n"
      ".Lsn13_%=:\n"
      "s_waitcnt vmcnt(0)\n"
      "s_mov_b64 exec, 0xff\n"
      "global_store_dwordx2 v126, v[124:125], %[ws] sc1\n"
      "s_mov_b64 exec, -1\n"
      "s_mov_b32 m0, s64\n"
      "s_cmp_eq_u32 s90, 0\n"
      "s_cbranch_scc1 .Lbdone_%=\n"
      "s_waitcnt lgkmcnt(0)\n"
      "s_branch .Lround_%=\n"
      ".Lsf2_%=:\n"
      "s_bitset1_b32 s90, 2\n"
      "global_load_dwordx4 v[72:75], v4, s[70:71] offset:0 sc1\n"
      "ds_read_b64_tr_b16 a[192:193], v16 offset:24576\n"
      "ds_read_b64_tr_b16 a[194:195], v16 offset:25600\n"
      "ds_read_b64_tr_b16 a[196:197], v17 offset:24576\n"
      "ds_read_b64_tr_b16 a[198:199], v17 offset:25600\n"
      "ds_read_b64_tr_b16 a[200:201], v18 offset:24576\n"
      "ds_read_b64_tr_b16 a[202:203], v18 offset:25600\n"
      "ds_read_b64_tr_b16 a[204:205], v19 offset:24576\n"
      "ds_read_b64_tr_b16 a[206:207], v19 offset:25600\n"
      "ds_read_b64_tr_b16 a[208:209], v16 offset:28672\n"
      "ds_read_b64_tr_b16 a[210:211], v16 offset:29696\n"
      "ds_read_b64_tr_b16 a[212:213], v17 offset:28672\n"
      "ds_read_b64_tr_b16 a[214:215], v17 offset:29696\n"
      "ds_read_b64_tr_b16 a[216:217], v18 offset:28672\n"
      "ds_read_b64_tr_b16 a[218:219], v18 offset:29696\n"
      "ds_read_b64_tr_b16 a[220:221], v19 offset:28672\n"
      "ds_read_b64_tr_b16 a[222:223], v19 offset:29696\n"
      "s_add_u32 s88, s88, 0x8000\n"
      "s_addc_u32 s89, s89, 0\n"
      "global_load_lds_dwordx4 v9, s[88:89]\n"
      "s_waitcnt vmcnt(12)\n"
      "v_max3_u32 v8, v76, v77, v78\n"
      "v_max_u32 v8, v8, v79\n"
      "v_cmp_eq_u32 vcc, -1, v8\n"
      "s_branch .Lsn2_%=\n"
      ".Lsf3_%=:\n"
      "s_bitset1_b32 s90, 3\n"
      "global_load_dwordx4 v[76:79], v4, s[70:71] offset:1024 sc1\n"
      "ds_read_b64_tr_b16 a[224:225], v16 offset:16384\n"
      "ds_read_b64_tr_b16 a[226:227], v16 offset:17408\n"
      "ds_read_b64_tr_b16 a[228:229], v17 offset:16384\n"
      "ds_read_b64_tr_b16 a[230:231], v17 offset:17408\n"
      "ds_read_b64_tr_b16 a[232:233], v18 offset:16384\n"
      "ds_read_b64_tr_b16 a[234:235], v18 offset:17408\n"
      "ds_read_b64_tr_b16 a[236:237], v19 offset:16384\n"
      "ds_read_b64_tr_b16 a[238:239], v19 offset:17408\n"
      "ds_read_b64_tr_b16 a[240:241], v16 offset:20480\n"
      "ds_read_b64_tr_b16 a[242:243], v16 offset:21504\n"
      "ds_read_b64_tr_b16 a[244:245], v17 offset:20480\n"
      "ds_read_b64_tr_b16 a[246:247], v17 offset:21504\n"
      "ds_read_b64_tr_b16 a[248:249], v18 offset:20480\n"
      "ds_read_b64_tr_b16 a[250:251], v18 offset:21504\n"
      "ds_read_b64_tr_b16 a[252:253], v19 offset:20480\n"
      "ds_read_b64_tr_b16 a[254:255], v19 offset:21504\n"
      "global_load_dwordx4 v[96:99], v4, s[76:77] offset:0 sc1\n"
      "global_load_dwordx4 v[100:103], v4, s[76:77] offset:1024 sc1\n"
      "global_load_dwordx4 v[104:107], v4, s[78:79] offset:0 sc1\n"
      "global_load_dwordx4 v[108:111], v4, s[78:79] offset:1024 sc1\n"
      "global_load_dwordx2 v[6:7], v5, %[ws] sc1\n"
      "s_waitcnt vmcnt(12)\n"
      "v_max3_u32 v8, v80, v81, v82\n"
      "v_max_u32 v8, v8, v83\n"
      "v_cmp_eq_u32 vcc, -1, v8\n"
      "s_branch .Lsn3_%=\n"
      ".Lsf4_%=:\n"
      "s_bitset1_b32 s90, 4\n"
      "global_load_dwordx4 v[80:83], v4, s[72:73] offset:0 sc1\n"
      "ds_read_b64_tr_b16 v[128:129], v16 offset:8192\n"
      "ds_read_b64_tr_b16 v[130:131], v16 offset:9216\n"
      "ds_read_b64_tr_b16 v[132:133], v17 offset:8192\n"
      "ds_read_b64_tr_b16 v[134:135], v17 offset:9216\n"
      "ds_read_b64_tr_b16 v[136:137], v18 offset:8192\n"
      "ds_read_b64_tr_b16 v[138:139], v18 offset:9216\n"
      "ds_read_b64_tr_b16 v[140:141], v19 offset:8192\n"
      "ds_read_b64_tr_b16 v[142:143], v19 offset:9216\n"
      "ds_read_b64_tr_b16 v[144:145], v16 offset:12288\n"
      "ds_read_b64_tr_b16 v[146:147], v16 offset:13312\n"
      "ds_read_b64_tr_b16 v[148:149], v17 offset:12288\n"
      "ds_read_b64_tr_b16 v[150:151], v17 offset:13312\n"
      "ds_read_b64_tr_b16 v[152:153], v18 offset:12288\n"
      "ds_read_b64_tr_b16 v[154:155], v18 offset:13312\n"
      "ds_read_b64_tr_b16 v[156:157], v19 offset:12288\n"
      "ds_read_b64_tr_b16 v[158:159], v19 offset:13312\n"
      "global_load_dwordx4 v[112:115], v4, s[80:81] offset:0 sc1\n"
      "s_waitcnt vmcnt(12)\n"
      "v_max3_u32 v8, v84, v85, v86\n"
      "v_max_u32 v8, v8, v87\n"
      "v_cmp_eq_u32 vcc, -1, v8\n"
      "s_branch .Lsn4_%=\n"
      ".Lsf5_%=:\n"
      "s_bitset1_b32 s90, 5\n"
      "global_load_dwordx4 v[84:87], v4, s[72:73] offset:1024 sc1\n"
      "ds_read_b64_tr_b16 v[160:161], v16 offset:0\n"
      "ds_read_b64_tr_b16 v[162:163], v16 offset:1024\n"
      "ds_read_b64_tr_b16 v[164:165], v17 offset:0\n"
      "ds_read_b64_tr_b16 v[166:167], v17 offset:1024\n"
      "ds_read_b64_tr_b16 v[168:169], v18 offset:0\n"
      "ds_read_b64_tr_b16 v[170:171], v18 offset:1024\n"
      "ds_read_b64_tr_b16 v[172:173], v19 offset:0\n"
      "ds_read_b64_tr_b16 v[174:175], v19 offset:1024\n"
      "ds_read_b64_tr_b16 v[176:177], v16 offset:4096\n"
      "ds_read_b64_tr_b16 v[178:179], v16 offset:5120\n"
      "ds_read_b64_tr_b16 v[180:181], v17 offset:4096\n"
      "ds_read_b64_tr_b16 v[182:183], v17 offset:5120\n"
      "ds_read_b64_tr_b16 v[184:185], v18 offset:4096\n"
      "ds_read_b64_tr_b16 v[186:187], v18 offset:5120\n"
      "ds_read_b64_tr_b16 v[188:189], v19 offset:4096\n"
      "ds_read_b64_tr_b16 v[190:191], v19 offset:5120\n"
      "global_load_dwordx4 v[116:119], v4, s[80:81] offset:1024 sc1\n"
      "s_waitcnt vmcnt(12)\n"
      "v_max3_u32 v8, v88, v89, v90\n"
      "v_max_u32 v8, v8, v91\n"
      "v_cmp_eq_u32 vcc, -1, v8\n"
      "s_branch .Lsn5_%=\n"
      ".Lsf6_%=:\n"
      "s_bitset1_b32 s90, 6\n"
      "global_load_dwordx4 v[88:91], v4, s[74:75] offset:0 sc1\n"
      "ds_read_b64_tr_b16 v[192:193], v12 offset:57344\n"
      "ds_read_b64_tr_b16 v[194:195], v12 offset:58368\n"
      "ds_read_b64_tr_b16 v[196:197], v13 offset:57344\n"
      "ds_read_b64_tr_b16 v[198:199], v13 offset:58368\n"
      "ds_read_b64_tr_b16 v[200:201], v14 offset:57344\n"
      "ds_read_b64_tr_b16 v[202:203], v14 offset:58368\n"
      "ds_read_b64_tr_b16 v[204:205], v15 offset:57344\n"
      "ds_read_b64_tr_b16 v[206:207], v15 offset:58368\n"
      "ds_read_b64_tr_b16 v[208:209], v12 offset:61440\n"
      "ds_read_b64_tr_b16 v[210:211], v12 offset:62464\n"
      "ds_read_b64_tr_b16 v[212:213], v13 offset:61440\n"
      "ds_read_b64_tr_b16 v[214:215], v13 offset:62464\n"
      "ds_read_b64_tr_b16 v[216:217], v14 offset:61440\n"
      "ds_read_b64_tr_b16 v[218:219], v14 offset:62464\n"
      "ds_read_b64_tr_b16 v[220:221], v15 offset:61440\n"
      "ds_read_b64_tr_b16 v[222:223], v15 offset:62464\n"
      "s_waitcnt vmcnt(15)\n"
      "v_max3_u32 v8, v92, v93, v94\n"
      "v_max_u32 v8, v8, v95\n"
      "v_cmp_eq_u32 vcc, -1, v8\n"
      "s_branch .Lsn6_%=\n"
      ".Lsf7_%=:\n"
      "s_bitset1_b32 s90, 7\n"
      "global_load_dwordx4 v[92:95], v4, s[74:75] offset:1024 sc1\n"
      "ds_read_b64_tr_b16 v[224:225], v12 offset:49152\n"
      "ds_read_b64_tr_b16 v[226:227], v12 offset:50176\n"
      "ds_read_b64_tr_b16 v[228:229], v13 offset:49152\n"
      "ds_read_b64_tr_b16 v[230:231], v13 offset:50176\n"
      "ds_read_b64_tr_b16 v[232:233], v14 offset:49152\n"
      "ds_read_b64_tr_b16 v[234:235], v14 offset:50176\n"
      "ds_read_b64_tr_b16 v[236:237], v15 offset:49152\n"
      "ds_read_b64_tr_b16 v[238:239], v15 offset:50176\n"
      "ds_read_b64_tr_b16 v[240:241], v12 offset:53248\n"
      "ds_read_b64_tr_b16 v[242:243], v12 offset:54272\n"
      "ds_read_b64_tr_b16 v[244:245], v13 offset:53248\n"
      "ds_read_b64_tr_b16 v[246:247], v13 offset:54272\n"
      "ds_read_b64_tr_b16 v[248:249], v14 offset:53248\n"
      "ds_read_b64_tr_b16 v[250:251], v14 offset:54272\n"
      "ds_read_b64_tr_b16 v[252:253], v15 offset:53248\n"
      "ds_read_b64_tr_b16 v[254:255], v15 offset:54272\n"
      "s_waitcnt vmcnt(10)\n"
      "v_max3_u32 v8, v96, v97, v98\n"
      "v_max_u32 v8, v8, v99\n"
      "v_cmp_eq_u32 vcc, -1, v8\n"
      "s_branch .Lsn7_%=\n"
      ".Lsf8_%=:\n"
      "s_bitset1_b32 s90, 8\n"
      "global_load_dwordx4 v[96:99], v4, s[76:77] offset:0 sc1\n"
      "s_waitcnt vmcnt(9)\n"
      "v_max3_u32 v8, v100, v101, v102\n"
      "v_max_u32 v8, v8, v103\n"
      "v_cmp_eq_u32 vcc, -1, v8\n"
      "s_branch .Lsn8_%=\n"
      ".Lsf9_%=:\n"
      "s_bitset1_b32 s90, 9\n"
      "global_load_dwordx4 v[100:103], v4, s[76:77] offset:1024 sc1\n"
      "s_waitcnt vmcnt(8)\n"
      "v_max3_u32 v8, v104, v105, v106\n"
      "v_max_u32 v8, v8, v107\n"
      "v_cmp_eq_u32 vcc, -1, v8\n"
      "s_branch .Lsn9_%=\n"
      ".Lsf10_%=:\n"
      "s_bitset1_b32 s90, 10\n"
      "global_load_dwordx4 v[104:107], v4, s[78:79] offset:0 sc1\n"
      "s_waitcnt vmcnt(7)\n"
      "v_max3_u32 v8, v108, v109, v110\n"
      "v_max_u32 v8, v8, v111\n"
      "v_cmp_eq_u32 vcc, -1, v8\n"
      "s_branch .Lsn10_%=\n"
      ".Lsf11_%=:\n"
      "s_bitset1_b32 s90, 11\n"
      "global_load_dwordx4 v[108:111], v4, s[78:79] offset:1024 sc1\n"
      "s_waitcnt vmcnt(5)\n"
      "v_max3_u32 v8, v112, v113, v114\n"
      "v_max_u32 v8, v8, v115\n"
      "v_cmp_eq_u32 vcc, -1, v8\n"
      "s_branch .Lsn11_%=\n"
      ".Lsf12_%=:\n"
      "s_bitset1_b32 s90, 12\n"
      "global_load_dwordx4 v[112:115], v4, s[80:81] offset:0 sc1\n"
      "s_waitcnt vmcnt(4)\n"
      "v_max3_u32 v8, v116, v117, v118\n"
      "v_max_u32 v8, v8, v119\n"
      "v_cmp_eq_u32 vcc, -1, v8\n"
      "s_branch .Lsn12_%=\n"
      ".Lsf13_%=:\n"
      "s_bitset1_b32 s90, 13\n"
      "global_load_dwordx4 v[116:119], v4, s[80:81] offset:1024 sc1\n"
      "s_branch .Lsn13_%=\n"
      ".Lspare_%=:\n"
      "s_mov_b32 s94, 0x5ac3e7b1\n"
      "s_mov_b32 s95, 0x9d2f\n"
      "s_mov_b32 s96, 0\n"
      "s_mov_b32 s62, 0xbfb8aa3b\n"
      "s_mov_b32 s63, 0xbfb8aa3b\n"
      "s_mov_b64 s[54:55], %[ws]\n"
      "v_lshlrev_b32 v4, 4, %[ln]\n"
      "s_mul_i32 s67, %[bb], 32768\n"
      "s_mul_i32 s86, %[w], 8192\n"
      "s_add_u32 s67, s67, s86\n"
      "s_add_u32 s67, s67, 2101248\n"
      "s_add_u32 s40, s54, s67\n"
      "s_addc_u32 s41, s55, 0\n"
      "s_lshl_b32 s85, %[mt4], 2\n"
      "s_add_u32 s85, s85, %[w]\n"
      "s_mul_i32 s67, s85, 112\n"
      "s_mul_i32 s84, %[xcd], 14\n"
      "s_add_u32 s67, s67, s84\n"
      "s_lshl_b32 s87, s67, 10\n"
      "s_add_u32 s50, s54, s87\n"
      "s_addc_u32 s51, s55, 0\n"
      "s_and_b32 s84, %[w], 1\n"
      "s_mul_i32 s84, s84, 113664\n"
      "s_sub_u32 s52, s50, s84\n"
      "s_subb_u32 s53, s51, 0\n"
      "s_lshl_b32 s67, %[j], 11\n"
      "s_add_u32 s42, s52, s67\n"
      "s_addc_u32 s43, s53, 0\n"
      "v_add_u32 v105, 0x1c000, v4\n"
      "s_lshl_b32 s84, %[w], 10\n"
      "s_and_b32 s67, %[w], 2\n"
      "s_lshl_b32 s67, s67, 10\n"
      "s_add_u32 s84, s84, s67\n"
      "s_add_u32 s84, s84, %[lb]\n"
      "s_add_u32 s84, s84, 0\n"
      "v_add_u32 v106, s84, v4\n"
      "s_add_u32 s84, %[m0b], 0\n"
      "v_add_u32 v107, s84, v4\n"
      "v_bfe_u32 v6, %[ln], 2, 3\n"
      "v_lshrrev_b32 v8, 5, %[ln]\n"
      "v_lshlrev_b32 v8, 11, v8\n"
      "v_lshl_add_u32 v8, v6, 7, v8\n"
      "v_and_b32 v10, 3, %[ln]\n"
      "v_lshl_add_u32 v8, v10, 3, v8\n"
      "v_add_u32 v8, %[lb], v8\n"
      "v_lshrrev_b32 v10, 1, v6\n"
      "v_add_u32 v11, 0, v10\n"
      "v_and_b32 v11, 3, v11\n"
      "v_lshl_add_u32 v12, v11, 5, v8\n"
      "v_add_u32 v16, 0x10000, v12\n"
      "v_add_u32 v20, 0x20000, v12\n"
      "v_add_u32 v11, 1, v10\n"
      "v_and_b32 v11, 3, v11\n"
      "v_lshl_add_u32 v13, v11, 5, v8\n"
      "v_add_u32 v17, 0x10000, v13\n"
      "v_add_u32 v21, 0x20000, v13\n"
      "v_add_u32 v11, 2, v10\n"
      "v_and_b32 v11, 3, v11\n"
      "v_lshl_add_u32 v14, v11, 5, v8\n"
      "v_add_u32 v18, 0x10000, v14\n"
      "v_add_u32 v22, 0x20000, v14\n"
      "v_add_u32 v11, 3, v10\n"
      "v_and_b32 v11, 3, v11\n"
      "v_lshl_add_u32 v15, v11, 5, v8\n"
      "v_add_u32 v19, 0x10000, v15\n"
      "v_add_u32 v23, 0x20000, v15\n"
      "s_lshl_b32 s84, %[w], 1\n"
      "v_add_u32 v11, s84, v10\n"
      "v_and_b32 v11, 3, v11\n"
      "v_lshl_add_u32 v98, v11, 5, v8\n"
      "v_add_u32 v100, 0x10000, v98\n"
      "v_add_u32 v102, 0x20000, v98\n"
      "v_add_u32 v11, s84, v10\n"
      "v_add_u32 v11, 1, v11\n"
      "v_and_b32 v11, 3, v11\n"
      "v_lshl_add_u32 v99, v11, 5, v8\n"
      "v_add_u32 v101, 0x10000, v99\n"
      "v_add_u32 v103, 0x20000, v99\n"
      "v_and_b32 v6, 7, %[ln]\n"
      "v_lshrrev_b32 v8, 4, %[ln]\n"
      "v_xor_b32 v8, v8, v6\n"
      "v_lshlrev_b32 v8, 4, v8\n"
      "v_lshl_add_u32 v8, v6, 7, v8\n"
      "v_bfe_u32 v10, %[ln], 3, 1\n"
      "v_lshl_add_u32 v8, v10, 10, v8\n"
      "s_and_b32 s84, %[w], 2\n"
      "s_lshl_b32 s84, s84, 11\n"
      "s_add_u32 s84, s84, %[lb]\n"
      "v_add_u32 v92, s84, v8\n"
      "v_xor_b32 v93, 64, v92\n"
      "v_add_u32 v94, 0x10000, v92\n"
      "v_add_u32 v95, 0x10000, v93\n"
      "v_add_u32 v96, 0x20000, v92\n"
      "v_add_u32 v97, 0x20000, v93\n"
      "v_lshrrev_b32 v6, 3, %[ln]\n"
      "v_bfe_u32 v8, %[ln], 1, 2\n"
      "v_lshrrev_b32 v10, 1, v6\n"
      "v_sub_u32 v8, v8, v10\n"
      "v_xor_b32 v8, %[xcd], v8\n"
      "v_and_b32 v8, 3, v8\n"
      "v_and_b32 v10, 1, %[ln]\n"
      "v_lshlrev_b32 v10, 4, v10\n"
      "v_lshl_add_u32 v8, v8, 5, v10\n"
      "v_lshl_add_u32 v8, v6, 11, v8\n"
      "s_and_b32 s67, %[w], 1\n"
      "s_lshl_b32 s67, s67, 14\n"
      "s_lshr_b32 s84, %[w], 1\n"
      "s_lshr_b32 s57, %[xcd], 2\n"
      "s_xor_b32 s84, s84, s57\n"
      "s_lshl_b32 s84, s84, 7\n"
      "s_add_u32 s67, s67, s84\n"
      "v_add_u32 v9, s67, v8\n"
      "s_mul_i32 s67, %[xcd], 0xe0000\n"
      "s_lshl_b32 s84, %[j], 8\n"
      "s_add_u32 s67, s67, s84\n"
      "s_mov_b64 s[44:45], %[wd]\n"
      "s_add_u32 s44, s44, s67\n"
      "s_addc_u32 s45, s45, 0\n"
      "s_lshl_b32 s56, %[j], 17\n"
      "s_cmp_eq_u32 s56, 0xe0000\n"
      "s_cselect_b32 s56, 0, s56\n"
      "s_lshr_b32 s57, s56, 6\n"
      "s_add_u32 s68, s50, s57\n"
      "s_addc_u32 s69, s51, 0\n"
      "s_add_u32 s57, s57, 0x800\n"
      "s_cmp_eq_u32 s57, 0x3800\n"
      "s_cselect_b32 s57, 0, s57\n"
      "s_add_u32 s70, s50, s57\n"
      "s_addc_u32 s71, s51, 0\n"
      "s_add_u32 s57, s57, 0x800\n"
      "s_cmp_eq_u32 s57, 0x3800\n"
      "s_cselect_b32 s57, 0, s57\n"
      "s_add_u32 s72, s50, s57\n"
      "s_addc_u32 s73, s51, 0\n"
      "s_add_u32 s57, s57, 0x800\n"
      "s_cmp_eq_u32 s57, 0x3800\n"
      "s_cselect_b32 s57, 0, s57\n"
      "s_add_u32 s74, s50, s57\n"
      "s_addc_u32 s75, s51, 0\n"
      "s_add_u32 s57, s57, 0x800\n"
      "s_cmp_eq_u32 s57, 0x3800\n"
      "s_cselect_b32 s57, 0, s57\n"
      "s_add_u32 s76, s50, s57\n"
      "s_addc_u32 s77, s51, 0\n"
      "s_add_u32 s57, s57, 0x800\n"
      "s_cmp_eq_u32 s57, 0x3800\n"
      "s_cselect_b32 s57, 0, s57\n"
      "s_add_u32 s78, s50, s57\n"
      "s_addc_u32 s79, s51, 0\n"
      "s_add_u32 s57, s57, 0x800\n"
      "s_cmp_eq_u32 s57, 0x3800\n"
      "s_cselect_b32 s57, 0, s57\n"
      "s_add_u32 s80, s50, s57\n"
      "s_addc_u32 s81, s51, 0\n"
      "s_lshr_b32 s67, %[bb], 3\n"
      "s_mul_i32 s67, s67, 262144\n"
      "s_add_u32 s67, s67, s86\n"
      "s_add_u32 s67, s67, 2097152\n"
      "s_add_u32 s46, s54, s67\n"
      "s_addc_u32 s47, s55, 0\n"
      "s_lshl_b32 s67, s85, 15\n"
      "s_lshl_b32 s84, %[j], 8\n"
      "s_add_u32 s67, s67, s84\n"
      "s_lshl_b32 s84, %[xcd], 5\n"
      "s_add_u32 s67, s67, s84\n"
      "s_mov_b64 s[48:49], %[out]\n"
      "s_add_u32 s48, s48, s67\n"
      "s_addc_u32 s49, s49, 0\n"
      "s_lshl_b32 s67, %[bb], 9\n"
      "s_lshl_b32 s84, %[w], 7\n"
      "s_add_u32 s67, s67, s84\n"
      "s_add_u32 s67, s67, 0xc00000\n"
      "v_and_b32 v8, 15, %[ln]\n"
      "v_lshl_add_u32 v5, v8, 3, s67\n"
      "s_andn2_b32 s67, %[bb], 7\n"
      "s_lshl_b32 s67, s67, 9\n"
      "s_add_u32 s67, s67, s84\n"
      "s_lshl_b32 s84, %[xcd], 3\n"
      "s_add_u32 s67, s67, s84\n"
      "s_add_u32 s67, s67, 0xc00040\n"
      "v_and_b32 v8, 7, %[ln]\n"
      "v_lshl_add_u32 v126, v8, 9, s67\n"
      "v_mov_b32 v124, s94\n"
      "s_lshl_b32 s67, s95, 16\n"
      "v_mov_b32 v125, s67\n"
      "v_mov_b32 v120, -1\n"
      "v_mov_b32 v121, -1\n"
      "v_mov_b32 v122, -1\n"
      "v_mov_b32 v123, -1\n"
      "s_lshl_b32 s67, %[mt4], 12\n"
      "s_lshl_b32 s84, %[w], 7\n"
      "s_add_u32 s67, s67, s84\n"
      "s_add_u32 s67, s67, 0xc00000\n"
      "s_lshl_b32 s85, %[xcd], 9\n"
      "s_add_u32 s85, s85, s67\n"
      "v_and_b32 v10, 7, %[ln]\n"
      "v_lshl_add_u32 v104, v10, 14, s85\n"
      "s_mov_b64 s[88:89], %[wq0]\n"
      "s_lshr_b32 s67, s88, 7\n"
      "s_sub_u32 s67, s67, 3\n"
      "s_and_b32 s67, s67, 7\n"
      "s_cmp_eq_u32 s67, 7\n"
      "s_cbranch_scc1 .Lnohelp_%=\n"
      "s_lshl_b32 s67, s67, 7\n"
      "s_sub_u32 s88, s88, s67\n"
      "s_subb_u32 s89, s89, 0\n"
      "s_and_b32 s67, %[w], 1\n"
      "s_mul_i32 s67, s67, 458752\n"
      "s_sub_u32 s67, 1376256, s67\n"
      "s_add_u32 s88, s88, s67\n"
      "s_addc_u32 s89, s89, 0\n"
      "v_mul_u32_u24 v8, 7168, %[ln]\n"
      "global_load_dword v72, v8, s[88:89]\n"
      "s_branch .Lhelped_%=\n"
      ".Lnohelp_%=:\n"
      "global_load_dword v72, v4, %[xq0]\n"
      ".Lhelped_%=:\n"
      "s_add_u32 s88, s52, 0x1000\n"
      "s_addc_u32 s89, s53, 0\n"
      "global_store_dwordx4 v4, v[120:123], s[88:89] offset:-4096 sc1\n"
      "global_store_dwordx4 v4, v[120:123], s[88:89] offset:-2048 sc1\n"
      "global_store_dwordx4 v4, v[120:123], s[88:89] offset:0 sc1\n"
      "global_store_dwordx4 v4, v[120:123], s[88:89] offset:2048 sc1\n"
      "s_add_u32 s88, s88, 0x2000\n"
      "s_addc_u32 s89, s89, 0\n"
      "global_store_dwordx4 v4, v[120:123], s[88:89] offset:-4096 sc1\n"
      "global_store_dwordx4 v4, v[120:123], s[88:89] offset:-2048 sc1\n"
      "global_store_dwordx4 v4, v[120:123], s[88:89] offset:0 sc1\n"
      "s_add_u32 s88, s88, 106496\n"
      "s_addc_u32 s89, s89, 0\n"
      "global_store_dwordx4 v4, v[120:123], s[88:89] offset:-4096 sc1\n"
      "global_store_dwordx4 v4, v[120:123], s[88:89] offset:-2048 sc1\n"
      "global_store_dwordx4 v4, v[120:123], s[88:89] offset:0 sc1\n"
      "global_store_dwordx4 v4, v[120:123], s[88:89] offset:2048 sc1\n"
      "s_add_u32 s88, s88, 0x2000\n"
      "s_addc_u32 s89, s89, 0\n"
      "global_store_dwordx4 v4, v[120:123], s[88:89] offset:-4096 sc1\n"
      "global_store_dwordx4 v4, v[120:123], s[88:89] offset:-2048 sc1\n"
      "global_store_dwordx4 v4, v[120:123], s[88:89] offset:0 sc1\n"
      "global_store_dwordx4 v4, v[120:123], s[40:41] offset:-4096 sc1\n"
      "global_store_dwordx4 v4, v[120:123], s[40:41] offset:-3072 sc1\n"
      "global_store_dwordx4 v4, v[120:123], s[40:41] offset:-2048 sc1\n"
      "global_store_dwordx4 v4, v[120:123], s[40:41] offset:-1024 sc1\n"
      "s_waitcnt vmcnt(0)\n"
      "s_mov_b64 exec, 0xff\n"
      "global_store_dwordx2 v104, v[124:125], %[ws] sc1\n"
      "global_store_dwordx2 v126, v[124:125], %[ws] sc1\n"
      "s_mov_b64 exec, -1\n"
      "s_lshl_b32 s84, %[mt4], 2\n"
      "s_add_u32 s84, s84, %[w]\n"
      "s_lshr_b32 s85, s84, 1\n"
      "s_and_b32 s84, s84, 1\n"
      "s_mul_i32 s67, %[xcd], 0xe0000\n"
      "s_lshl_b32 s84, s84, 16\n"
      "s_add_u32 s67, s67, s84\n"
      "s_lshl_b32 s84, s85, 8\n"
      "s_add_u32 s67, s67, s84\n"
      "s_mov_b64 s[88:89], %[wd]\n"
      "s_add_u32 s88, s88, s67\n"
      "s_addc_u32 s89, s89, 0\n"
      "s_lshl_b32 s85, s85, 17\n"
      "s_cmp_eq_u32 s85, 0xe0000\n"
      "s_cselect_b32 s85, 0, s85\n"
      "v_lshrrev_b32 v8, 1, %[ln]\n"
      "v_and_b32 v10, 1, %[ln]\n"
      "v_lshlrev_b32 v10, 7, v10\n"
      "v_lshl_add_u32 v8, v8, 11, v10\n"
      "s_add_u32 s85, s85, 0x20000\n"
      "s_cmp_eq_u32 s85, 0xe0000\n"
      "s_cselect_b32 s85, 0, s85\n"
      "s_add_u32 s85, s85, 0x20000\n"
      "s_cmp_eq_u32 s85, 0xe0000\n"
      "s_cselect_b32 s85, 0, s85\n"
      "s_add_u32 s92, s88, s85\n"
      "s_addc_u32 s93, s89, 0\n"
      "global_load_dword v66, v8, s[92:93]\n"
      "s_add_u32 s85, s85, 0x20000\n"
      "s_cmp_eq_u32 s85, 0xe0000\n"
      "s_cselect_b32 s85, 0, s85\n"
      "s_add_u32 s92, s88, s85\n"
      "s_addc_u32 s93, s89, 0\n"
      "global_load_dword v67, v8, s[92:93]\n"
      "s_add_u32 s85, s85, 0x20000\n"
      "s_cmp_eq_u32 s85, 0xe0000\n"
      "s_cselect_b32 s85, 0, s85\n"
      "s_add_u32 s92, s88, s85\n"
      "s_addc_u32 s93, s89, 0\n"
      "global_load_dword v68, v8, s[92:93]\n"
      "s_add_u32 s85, s85, 0x20000\n"
      "s_cmp_eq_u32 s85, 0xe0000\n"
      "s_cselect_b32 s85, 0, s85\n"
      "s_add_u32 s92, s88, s85\n"
      "s_addc_u32 s93, s89, 0\n"
      "global_load_dword v69, v8, s[92:93]\n"
      "s_add_u32 s85, s85, 0x20000\n"
      "s_cmp_eq_u32 s85, 0xe0000\n"
      "s_cselect_b32 s85, 0, s85\n"
      "s_add_u32 s92, s88, s85\n"
      "s_addc_u32 s93, s89, 0\n"
      "global_load_dword v70, v8, s[92:93]\n"
      "s_add_u32 m0, %[m0b], 155648\n"
      "s_nop 0\n"
      "s_add_u32 s88, s44, s56\n"
      "s_addc_u32 s89, s45, 0\n"
      "s_add_u32 s56, s56, 0x10000\n"
      "s_cmp_eq_u32 s56, 0xe0000\n"
      "s_cselect_b32 s56, 0, s56\n"
      "global_load_lds_dwordx4 v9, s[88:89]\n"
      "s_add_u32 m0, %[m0b], 156672\n"
      "s_add_u32 s88, s88, 0x8000\n"
      "s_addc_u32 s89, s89, 0\n"
      "global_load_lds_dwordx4 v9, s[88:89]\n"
      "s_add_u32 m0, %[m0b], 147456\n"
      "s_add_u32 s88, s44, s56\n"
      "s_addc_u32 s89, s45, 0\n"
      "s_add_u32 s56, s56, 0x10000\n"
      "s_cmp_eq_u32 s56, 0xe0000\n"
      "s_cselect_b32 s56, 0, s56\n"
      "global_load_lds_dwordx4 v9, s[88:89]\n"
      "s_add_u32 m0, %[m0b], 148480\n"
      "s_add_u32 s88, s88, 0x8000\n"
      "s_addc_u32 s89, s89, 0\n"
      "global_load_lds_dwordx4 v9, s[88:89]\n"
      "s_add_u32 m0, %[m0b], 139264\n"
      "s_add_u32 s88, s44, s56\n"
      "s_addc_u32 s89, s45, 0\n"
      "s_add_u32 s56, s56, 0x10000\n"
      "s_cmp_eq_u32 s56, 0xe0000\n"
      "s_cselect_b32 s56, 0, s56\n"
      "global_load_lds_dwordx4 v9, s[88:89]\n"
      "s_add_u32 m0, %[m0b], 140288\n"
      "s_add_u32 s88, s88, 0x8000\n"
      "s_addc_u32 s89, s89, 0\n"
      "global_load_lds_dwordx4 v9, s[88:89]\n"
      "s_add_u32 m0, %[m0b], 131072\n"
      "s_add_u32 s88, s44, s56\n"
      "s_addc_u32 s89, s45, 0\n"
      "s_add_u32 s56, s56, 0x10000\n"
      "s_cmp_eq_u32 s56, 0xe0000\n"
      "s_cselect_b32 s56, 0, s56\n"
      "global_load_lds_dwordx4 v9, s[88:89]\n"
      "s_add_u32 m0, %[m0b], 132096\n"
      "s_add_u32 s88, s88, 0x8000\n"
      "s_addc_u32 s89, s89, 0\n"
      "global_load_lds_dwordx4 v9, s[88:89]\n"
      "s_add_u32 m0, %[m0b], 122880\n"
      "s_add_u32 s88, s44, s56\n"
      "s_addc_u32 s89, s45, 0\n"
      "s_add_u32 s56, s56, 0x10000\n"
      "s_cmp_eq_u32 s56, 0xe0000\n"
      "s_cselect_b32 s56, 0, s56\n"
      "global_load_lds_dwordx4 v9, s[88:89]\n"
      "s_add_u32 m0, %[m0b], 123904\n"
      "s_add_u32 s88, s88, 0x8000\n"
      "s_addc_u32 s89, s89, 0\n"
      "global_load_lds_dwordx4 v9, s[88:89]\n"
      "s_add_u32 m0, %[m0b], 114688\n"
      "s_add_u32 s88, s44, s56\n"
      "s_addc_u32 s89, s45, 0\n"
      "s_add_u32 s56, s56, 0x10000\n"
      "s_cmp_eq_u32 s56, 0xe0000\n"
      "s_cselect_b32 s56, 0, s56\n"
      "global_load_lds_dwordx4 v9, s[88:89]\n"
      "s_add_u32 m0, %[m0b], 115712\n"
      "s_add_u32 s88, s88, 0x8000\n"
      "s_addc_u32 s89, s89, 0\n"
      "global_load_lds_dwordx4 v9, s[88:89]\n"
      "s_add_u32 m0, %[m0b], 106496\n"
      "s_add_u32 s88, s44, s56\n"
      "s_addc_u32 s89, s45, 0\n"
      "s_add_u32 s56, s56, 0x10000\n"
      "s_cmp_eq_u32 s56, 0xe0000\n"
      "s_cselect_b32 s56, 0, s56\n"
      "global_load_lds_dwordx4 v9, s[88:89]\n"
      "s_add_u32 m0, %[m0b], 107520\n"
      "s_add_u32 s88, s88, 0x8000\n"
      "s_addc_u32 s89, s89, 0\n"
      "global_load_lds_dwordx4 v9, s[88:89]\n"
      "s_add_u32 m0, %[m0b], 98304\n"
      "s_add_u32 s88, s44, s56\n"
      "s_addc_u32 s89, s45, 0\n"
      "s_add_u32 s56, s56, 0x10000\n"
      "s_cmp_eq_u32 s56, 0xe0000\n"
      "s_cselect_b32 s56, 0, s56\n"
      "global_load_lds_dwordx4 v9, s[88:89]\n"
      "s_add_u32 m0, %[m0b], 99328\n"
      "s_add_u32 s88, s88, 0x8000\n"
      "s_addc_u32 s89, s89, 0\n"
      "global_load_lds_dwordx4 v9, s[88:89]\n"
      "s_add_u32 m0, %[m0b], 90112\n"
      "s_add_u32 s88, s44, s56\n"
      "s_addc_u32 s89, s45, 0\n"
      "s_add_u32 s56, s56, 0x10000\n"
      "s_cmp_eq_u32 s56, 0xe0000\n"
      "s_cselect_b32 s56, 0, s56\n"
      "global_load_lds_dwordx4 v9, s[88:89]\n"
      "s_add_u32 m0, %[m0b], 91136\n"
      "s_add_u32 s88, s88, 0x8000\n"
      "s_addc_u32 s89, s89, 0\n"
      "global_load_lds_dwordx4 v9, s[88:89]\n"
      "s_add_u32 m0, %[m0b], 81920\n"
      "s_add_u32 s88, s44, s56\n"
      "s_addc_u32 s89, s45, 0\n"
      "s_add_u32 s56, s56, 0x10000\n"
      "s_cmp_eq_u32 s56, 0xe0000\n"
      "s_cselect_b32 s56, 0, s56\n"
      "global_load_lds_dwordx4 v9, s[88:89]\n"
      "s_add_u32 m0, %[m0b], 82944\n"
      "s_add_u32 s88, s88, 0x8000\n"
      "s_addc_u32 s89, s89, 0\n"
      "global_load_lds_dwordx4 v9, s[88:89]\n"
      "s_add_u32 m0, %[m0b], 73728\n"
      "s_add_u32 s88, s44, s56\n"
      "s_addc_u32 s89, s45, 0\n"
      "s_add_u32 s56, s56, 0x10000\n"
      "s_cmp_eq_u32 s56, 0xe0000\n"
      "s_cselect_b32 s56, 0, s56\n"
      "global_load_lds_dwordx4 v9, s[88:89]\n"
      "s_add_u32 m0, %[m0b], 74752\n"
      "s_add_u32 s88, s88, 0x8000\n"
      "s_addc_u32 s89, s89, 0\n"
      "global_load_lds_dwordx4 v9, s[88:89]\n"
      "s_add_u32 m0, %[m0b], 65536\n"
      "s_add_u32 s88, s44, s56\n"
      "s_addc_u32 s89, s45, 0\n"
      "s_add_u32 s56, s56, 0x10000\n"
      "s_cmp_eq_u32 s56, 0xe0000\n"
      "s_cselect_b32 s56, 0, s56\n"
      "global_load_lds_dwordx4 v9, s[88:89]\n"
      "s_add_u32 m0, %[m0b], 66560\n"
      "s_add_u32 s88, s88, 0x8000\n"
      "s_addc_u32 s89, s89, 0\n"
      "global_load_lds_dwordx4 v9, s[88:89]\n"
      "s_add_u32 m0, %[m0b], 57344\n"
      "s_add_u32 s88, s44, s56\n"
      "s_addc_u32 s89, s45, 0\n"
      "s_add_u32 s56, s56, 0x10000\n"
      "s_cmp_eq_u32 s56, 0xe0000\n"
      "s_cselect_b32 s56, 0, s56\n"
      "global_load_lds_dwordx4 v9, s[88:89]\n"
      "s_add_u32 m0, %[m0b], 58368\n"
      "s_add_u32 s88, s88, 0x8000\n"
      "s_addc_u32 s89, s89, 0\n"
      "global_load_lds_dwordx4 v9, s[88:89]\n"
      "s_add_u32 m0, %[m0b], 49152\n"
      "s_add_u32 s88, s44, s56\n"
      "s_addc_u32 s89, s45, 0\n"
      "s_add_u32 s56, s56, 0x10000\n"
      "s_cmp_eq_u32 s56, 0xe0000\n"
      "s_cselect_b32 s56, 0, s56\n"
      "global_load_lds_dwordx4 v9, s[88:89]\n"
      "s_add_u32 m0, %[m0b], 50176\n"
      "s_add_u32 s88, s88, 0x8000\n"
      "s_addc_u32 s89, s89, 0\n"
      "global_load_lds_dwordx4 v9, s[88:89]\n"
      "s_mov_b32 m0, s64\n"
      "s_waitcnt vmcnt(0)\n"
      "s_barrier\n"
      "ds_read_b64_tr_b16 a[0:1], v20 offset:8192\n"
      "ds_read_b64_tr_b16 a[2:3], v20 offset:9216\n"
      "ds_read_b64_tr_b16 a[4:5], v21 offset:8192\n"
      "ds_read_b64_tr_b16 a[6:7], v21 offset:9216\n"
      "ds_read_b64_tr_b16 a[8:9], v22 offset:8192\n"
      "ds_read_b64_tr_b16 a[10:11], v22 offset:9216\n"
      "ds_read_b64_tr_b16 a[12:13], v23 offset:8192\n"
      "ds_read_b64_tr_b16 a[14:15], v23 offset:9216\n"
      "ds_read_b64_tr_b16 a[16:17], v20 offset:12288\n"
      "ds_read_b64_tr_b16 a[18:19], v20 offset:13312\n"
      "ds_read_b64_tr_b16 a[20:21], v21 offset:12288\n"
      "ds_read_b64_tr_b16 a[22:23], v21 offset:13312\n"
      "ds_read_b64_tr_b16 a[24:25], v22 offset:12288\n"
      "ds_read_b64_tr_b16 a[26:27], v22 offset:13312\n"
      "ds_read_b64_tr_b16 a[28:29], v23 offset:12288\n"
      "ds_read_b64_tr_b16 a[30:31], v23 offset:13312\n"
      "ds_read_b64_tr_b16 a[32:33], v20 offset:0\n"
      "ds_read_b64_tr_b16 a[34:35], v20 offset:1024\n"
      "ds_read_b64_tr_b16 a[36:37], v21 offset:0\n"
      "ds_read_b64_tr_b16 a[38:39], v21 offset:1024\n"
      "ds_read_b64_tr_b16 a[40:41], v22 offset:0\n"
      "ds_read_b64_tr_b16 a[42:43], v22 offset:1024\n"
      "ds_read_b64_tr_b16 a[44:45], v23 offset:0\n"
      "ds_read_b64_tr_b16 a[46:47], v23 offset:1024\n"
      "ds_read_b64_tr_b16 a[48:49], v20 offset:4096\n"
      "ds_read_b64_tr_b16 a[50:51], v20 offset:5120\n"
      "ds_read_b64_tr_b16 a[52:53], v21 offset:4096\n"
      "ds_read_b64_tr_b16 a[54:55], v21 offset:5120\n"
      "ds_read_b64_tr_b16 a[56:57], v22 offset:4096\n"
      "ds_read_b64_tr_b16 a[58:59], v22 offset:5120\n"
      "ds_read_b64_tr_b16 a[60:61], v23 offset:4096\n"
      "ds_read_b64_tr_b16 a[62:63], v23 offset:5120\n"
      "ds_read_b64_tr_b16 a[64:65], v16 offset:57344\n"
      "ds_read_b64_tr_b16 a[66:67], v16 offset:58368\n"
      "ds_read_b64_tr_b16 a[68:69], v17 offset:57344\n"
      "ds_read_b64_tr_b16 a[70:71], v17 offset:58368\n"
      "ds_read_b64_tr_b16 a[72:73], v18 offset:57344\n"
      "ds_read_b64_tr_b16 a[74:75], v18 offset:58368\n"
      "ds_read_b64_tr_b16 a[76:77], v19 offset:57344\n"
      "ds_read_b64_tr_b16 a[78:79], v19 offset:58368\n"
      "ds_read_b64_tr_b16 a[80:81], v16 offset:61440\n"
      "ds_read_b64_tr_b16 a[82:83], v16 offset:62464\n"
      "ds_read_b64_tr_b16 a[84:85], v17 offset:61440\n"
      "ds_read_b64_tr_b16 a[86:87], v17 offset:62464\n"
      "ds_read_b64_tr_b16 a[88:89], v18 offset:61440\n"
      "ds_read_b64_tr_b16 a[90:91], v18 offset:62464\n"
      "ds_read_b64_tr_b16 a[92:93], v19 offset:61440\n"
      "ds_read_b64_tr_b16 a[94:95], v19 offset:62464\n"
      "ds_read_b64_tr_b16 a[96:97], v16 offset:49152\n"
      "ds_read_b64_tr_b16 a[98:99], v16 offset:50176\n"
      "ds_read_b64_tr_b16 a[100:101], v17 offset:49152\n"
      "ds_read_b64_tr_b16 a[102:103], v17 offset:50176\n"
      "ds_read_b64_tr_b16 a[104:105], v18 offset:49152\n"
      "ds_read_b64_tr_b16 a[106:107], v18 offset:50176\n"
      "ds_read_b64_tr_b16 a[108:109], v19 offset:49152\n"
      "ds_read_b64_tr_b16 a[110:111], v19 offset:50176\n"
      "ds_read_b64_tr_b16 a[112:113], v16 offset:53248\n"
      "ds_read_b64_tr_b16 a[114:115], v16 offset:54272\n"
      "ds_read_b64_tr_b16 a[116:117], v17 offset:53248\n"
      "ds_read_b64_tr_b16 a[118:119], v17 offset:54272\n"
      "ds_read_b64_tr_b16 a[120:121], v18 offset:53248\n"
      "ds_read_b64_tr_b16 a[122:123], v18 offset:54272\n"
      "ds_read_b64_tr_b16 a[124:125], v19 offset:53248\n"
      "ds_read_b64_tr_b16 a[126:127], v19 offset:54272\n"
      "ds_read_b64_tr_b16 a[128:129], v16 offset:40960\n"
      "ds_read_b64_tr_b16 a[130:131], v16 offset:41984\n"
      "ds_read_b64_tr_b16 a[132:133], v17 offset:40960\n"
      "ds_read_b64_tr_b16 a[134:135], v17 offset:41984\n"
      "ds_read_b64_tr_b16 a[136:137], v18 offset:40960\n"
      "ds_read_b64_tr_b16 a[138:139], v18 offset:41984\n"
      "ds_read_b64_tr_b16 a[140:141], v19 offset:40960\n"
      "ds_read_b64_tr_b16 a[142:143], v19 offset:41984\n"
      "ds_read_b64_tr_b16 a[144:145], v16 offset:45056\n"
      "ds_read_b64_tr_b16 a[146:147], v16 offset:46080\n"
      "ds_read_b64_tr_b16 a[148:149], v17 offset:45056\n"
      "ds_read_b64_tr_b16 a[150:151], v17 offset:46080\n"
      "ds_read_b64_tr_b16 a[152:153], v18 offset:45056\n"
      "ds_read_b64_tr_b16 a[154:155], v18 offset:46080\n"
      "ds_read_b64_tr_b16 a[156:157], v19 offset:45056\n"
      "ds_read_b64_tr_b16 a[158:159], v19 offset:46080\n"
      "ds_read_b64_tr_b16 a[160:161], v16 offset:32768\n"
      "ds_read_b64_tr_b16 a[162:163], v16 offset:33792\n"
      "ds_read_b64_tr_b16 a[164:165], v17 offset:32768\n"
      "ds_read_b64_tr_b16 a[166:167], v17 offset:33792\n"
      "ds_read_b64_tr_b16 a[168:169], v18 offset:32768\n"
      "ds_read_b64_tr_b16 a[170:171], v18 offset:33792\n"
      "ds_read_b64_tr_b16 a[172:173], v19 offset:32768\n"
      "ds_read_b64_tr_b16 a[174:175], v19 offset:33792\n"
      "ds_read_b64_tr_b16 a[176:177], v16 offset:36864\n"
      "ds_read_b64_tr_b16 a[178:179], v16 offset:37888\n"
      "ds_read_b64_tr_b16 a[180:181], v17 offset:36864\n"
      "ds_read_b64_tr_b16 a[182:183], v17 offset:37888\n"
      "ds_read_b64_tr_b16 a[184:185], v18 offset:36864\n"
      "ds_read_b64_tr_b16 a[186:187], v18 offset:37888\n"
      "ds_read_b64_tr_b16 a[188:189], v19 offset:36864\n"
      "ds_read_b64_tr_b16 a[190:191], v19 offset:37888\n"
      "ds_read_b64_tr_b16 a[192:193], v16 offset:24576\n"
      "ds_read_b64_tr_b16 a[194:195], v16 offset:25600\n"
      "ds_read_b64_tr_b16 a[196:197], v17 offset:24576\n"
      "ds_read_b64_tr_b16 a[198:199], v17 offset:25600\n"
      "ds_read_b64_tr_b16 a[200:201], v18 offset:24576\n"
      "ds_read_b64_tr_b16 a[202:203], v18 offset:25600\n"
      "ds_read_b64_tr_b16 a[204:205], v19 offset:24576\n"
      "ds_read_b64_tr_b16 a[206:207], v19 offset:25600\n"
      "ds_read_b64_tr_b16 a[208:209], v16 offset:28672\n"
      "ds_read_b64_tr_b16 a[210:211], v16 offset:29696\n"
      "ds_read_b64_tr_b16 a[212:213], v17 offset:28672\n"
      "ds_read_b64_tr_b16 a[214:215], v17 offset:29696\n"
      "ds_read_b64_tr_b16 a[216:217], v18 offset:28672\n"
      "ds_read_b64_tr_b16 a[218:219], v18 offset:29696\n"
      "ds_read_b64_tr_b16 a[220:221], v19 offset:28672\n"
      "ds_read_b64_tr_b16 a[222:223], v19 offset:29696\n"
      "ds_read_b64_tr_b16 a[224:225], v16 offset:16384\n"
      "ds_read_b64_tr_b16 a[226:227], v16 offset:17408\n"
      "ds_read_b64_tr_b16 a[228:229], v17 offset:16384\n"
      "ds_read_b64_tr_b16 a[230:231], v17 offset:17408\n"
      "ds_read_b64_tr_b16 a[232:233], v18 offset:16384\n"
      "ds_read_b64_tr_b16 a[234:235], v18 offset:17408\n"
      "ds_read_b64_tr_b16 a[236:237], v19 offset:16384\n"
      "ds_read_b64_tr_b16 a[238:239], v19 offset:17408\n"
      "ds_read_b64_tr_b16 a[240:241], v16 offset:20480\n"
      "ds_read_b64_tr_b16 a[242:243], v16 offset:21504\n"
      "ds_read_b64_tr_b16 a[244:245], v17 offset:20480\n"
      "ds_read_b64_tr_b16 a[246:247], v17 offset:21504\n"
      "ds_read_b64_tr_b16 a[248:249], v18 offset:20480\n"
      "ds_read_b64_tr_b16 a[250:251], v18 offset:21504\n"
      "ds_read_b64_tr_b16 a[252:253], v19 offset:20480\n"
      "ds_read_b64_tr_b16 a[254:255], v19 offset:21504\n"
      "ds_read_b64_tr_b16 v[128:129], v16 offset:8192\n"
      "ds_read_b64_tr_b16 v[130:131], v16 offset:9216\n"
      "ds_read_b64_tr_b16 v[132:133], v17 offset:8192\n"
      "ds_read_b64_tr_b16 v[134:135], v17 offset:9216\n"
      "ds_read_b64_tr_b16 v[136:137], v18 offset:8192\n"
      "ds_read_b64_tr_b16 v[138:139], v18 offset:9216\n"
      "ds_read_b64_tr_b16 v[140:141], v19 offset:8192\n"
      "ds_read_b64_tr_b16 v[142:143], v19 offset:9216\n"
      "ds_read_b64_tr_b16 v[144:145], v16 offset:12288\n"
      "ds_read_b64_tr_b16 v[146:147], v16 offset:13312\n"
      "ds_read_b64_tr_b16 v[148:149], v17 offset:12288\n"
      "ds_read_b64_tr_b16 v[150:151], v17 offset:13312\n"
      "ds_read_b64_tr_b16 v[152:153], v18 offset:12288\n"
      "ds_read_b64_tr_b16 v[154:155], v18 offset:13312\n"
      "ds_read_b64_tr_b16 v[156:157], v19 offset:12288\n"
      "ds_read_b64_tr_b16 v[158:159], v19 offset:13312\n"
      "global_load_dwordx2 v[6:7], v5, %[ws] sc1\n"
      "global_load_dwordx4 v[64:67], v4, s[68:69] offset:0 sc1\n"
      "global_load_dwordx4 v[68:71], v4, s[68:69] offset:1024 sc1\n"
      "global_load_dwordx4 v[72:75], v4, s[70:71] offset:0 sc1\n"
      "global_load_dwordx4 v[76:79], v4, s[70:71] offset:1024 sc1\n"
      "global_load_dwordx4 v[80:83], v4, s[72:73] offset:0 sc1\n"
      "global_load_dwordx4 v[84:87], v4, s[72:73] offset:1024 sc1\n"
      "global_load_dwordx4 v[88:91], v4, s[74:75] offset:0 sc1\n"
      "global_load_dwordx4 v[92:95], v4, s[74:75] offset:1024 sc1\n"
      "global_load_dwordx4 v[96:99], v4, s[76:77] offset:0 sc1\n"
      "global_load_dwordx4 v[100:103], v4, s[76:77] offset:1024 sc1\n"
      "global_load_dwordx4 v[104:107], v4, s[78:79] offset:0 sc1\n"
      "global_load_dwordx4 v[108:111], v4, s[78:79] offset:1024 sc1\n"
      "global_load_dwordx4 v[112:115], v4, s[80:81] offset:0 sc1\n"
      "global_load_dwordx4 v[116:119], v4, s[80:81] offset:1024 sc1\n"
      "ds_read_b64_tr_b16 v[160:161], v16 offset:0\n"
      "ds_read_b64_tr_b16 v[162:163], v16 offset:1024\n"
      "ds_read_b64_tr_b16 v[164:165], v17 offset:0\n"
      "ds_read_b64_tr_b16 v[166:167], v17 offset:1024\n"
      "ds_read_b64_tr_b16 v[168:169], v18 offset:0\n"
      "ds_read_b64_tr_b16 v[170:171], v18 offset:1024\n"
      "ds_read_b64_tr_b16 v[172:173], v19 offset:0\n"
      "ds_read_b64_tr_b16 v[174:175], v19 offset:1024\n"
      "ds_read_b64_tr_b16 v[176:177], v16 offset:4096\n"
      "ds_read_b64_tr_b16 v[178:179], v16 offset:5120\n"
      "ds_read_b64_tr_b16 v[180:181], v17 offset:4096\n"
      "ds_read_b64_tr_b16 v[182:183], v17 offset:5120\n"
      "ds_read_b64_tr_b16 v[184:185], v18 offset:4096\n"
      "ds_read_b64_tr_b16 v[186:187], v18 offset:5120\n"
      "ds_read_b64_tr_b16 v[188:189], v19 offset:4096\n"
      "ds_read_b64_tr_b16 v[190:191], v19 offset:5120\n"
      "ds_read_b64_tr_b16 v[192:193], v12 offset:57344\n"
      "ds_read_b64_tr_b16 v[194:195], v12 offset:58368\n"
      "ds_read_b64_tr_b16 v[196:197], v13 offset:57344\n"
      "ds_read_b64_tr_b16 v[198:199], v13 offset:58368\n"
      "ds_read_b64_tr_b16 v[200:201], v14 offset:57344\n"
      "ds_read_b64_tr_b16 v[202:203], v14 offset:58368\n"
      "ds_read_b64_tr_b16 v[204:205], v15 offset:57344\n"
      "ds_read_b64_tr_b16 v[206:207], v15 offset:58368\n"
      "ds_read_b64_tr_b16 v[208:209], v12 offset:61440\n"
      "ds_read_b64_tr_b16 v[210:211], v12 offset:62464\n"
      "ds_read_b64_tr_b16 v[212:213], v13 offset:61440\n"
      "ds_read_b64_tr_b16 v[214:215], v13 offset:62464\n"
      "ds_read_b64_tr_b16 v[216:217], v14 offset:61440\n"
      "ds_read_b64_tr_b16 v[218:219], v14 offset:62464\n"
      "ds_read_b64_tr_b16 v[220:221], v15 offset:61440\n"
      "ds_read_b64_tr_b16 v[222:223], v15 offset:62464\n"
      "ds_read_b64_tr_b16 v[224:225], v12 offset:49152\n"
      "ds_read_b64_tr_b16 v[226:227], v12 offset:50176\n"
      "ds_read_b64_tr_b16 v[228:229], v13 offset:49152\n"
      "ds_read_b64_tr_b16 v[230:231], v13 offset:50176\n"
      "ds_read_b64_tr_b16 v[232:233], v14 offset:49152\n"
      "ds_read_b64_tr_b16 v[234:235], v14 offset:50176\n"
      "ds_read_b64_tr_b16 v[236:237], v15 offset:49152\n"
      "ds_read_b64_tr_b16 v[238:239], v15 offset:50176\n"
      "ds_read_b64_tr_b16 v[240:241], v12 offset:53248\n"
      "ds_read_b64_tr_b16 v[242:243], v12 offset:54272\n"
      "ds_read_b64_tr_b16 v[244:245], v13 offset:53248\n"
      "ds_read_b64_tr_b16 v[246:247], v13 offset:54272\n"
      "ds_read_b64_tr_b16 v[248:249], v14 offset:53248\n"
      "ds_read_b64_tr_b16 v[250:251], v14 offset:54272\n"
      "ds_read_b64_tr_b16 v[252:253], v15 offset:53248\n"
      "ds_read_b64_tr_b16 v[254:255], v15 offset:54272\n"
      "ds_read_b64_tr_b16 v[24:25], v20 offset:24576\n"
      "ds_read_b64_tr_b16 v[26:27], v20 offset:25600\n"
      "ds_read_b64_tr_b16 v[28:29], v21 offset:24576\n"
      "ds_read_b64_tr_b16 v[30:31], v21 offset:25600\n"
      "ds_read_b64_tr_b16 v[120:121], v22 offset:24576\n"
      "ds_read_b64_tr_b16 v[122:123], v22 offset:25600\n"
      "ds_read_b64_tr_b16 v[124:125], v23 offset:24576\n"
      "ds_read_b64_tr_b16 v[126:127], v23 offset:25600\n"
      "v_mov_b32 v32, 0\n"
      "v_mov_b32 v33, 0\n"
      "v_mov_b32 v34, 0\n"
      "v_mov_b32 v35, 0\n"
      "v_mov_b32 v36, 0\n"
      "v_mov_b32 v37, 0\n"
      "v_mov_b32 v38, 0\n"
      "v_mov_b32 v39, 0\n"
      "v_mov_b32 v40, 0\n"
      "v_mov_b32 v41, 0\n"
      "v_mov_b32 v42, 0\n"
      "v_mov_b32 v43, 0\n"
      "v_mov_b32 v44, 0\n"
      "v_mov_b32 v45, 0\n"
      "v_mov_b32 v46, 0\n"
      "v_mov_b32 v47, 0\n"
      "v_mov_b32 v48, 0\n"
      "v_mov_b32 v49, 0\n"
      "v_mov_b32 v50, 0\n"
      "v_mov_b32 v51, 0\n"
      "v_mov_b32 v52, 0\n"
      "v_mov_b32 v53, 0\n"
      "v_mov_b32 v54, 0\n"
      "v_mov_b32 v55, 0\n"
      "v_mov_b32 v56, 0\n"
      "v_mov_b32 v57, 0\n"
      "v_mov_b32 v58, 0\n"
      "v_mov_b32 v59, 0\n"
      "v_mov_b32 v60, 0\n"
      "v_mov_b32 v61, 0\n"
      "v_mov_b32 v62, 0\n"
      "v_mov_b32 v63, 0\n"
      "v_lshlrev_b32 v10, 3, %[ln]\n"
      "s_xor_b32 s67, %[xcd], 1\n"
      "s_mul_i32 s67, s67, 32768\n"
      "s_add_u32 s67, s67, 512\n"
      "v_add_u32 v12, s67, v10\n"
      "s_xor_b32 s67, %[xcd], 2\n"
      "s_mul_i32 s67, s67, 32768\n"
      "s_add_u32 s67, s67, 1024\n"
      "v_add_u32 v13, s67, v10\n"
      "s_xor_b32 s67, %[xcd], 3\n"
      "s_mul_i32 s67, s67, 32768\n"
      "s_add_u32 s67, s67, 1536\n"
      "v_add_u32 v14, s67, v10\n"
      "s_xor_b32 s67, %[xcd], 4\n"
      "s_mul_i32 s67, s67, 32768\n"
      "s_add_u32 s67, s67, 2048\n"
      "v_add_u32 v15, s67, v10\n"
      "s_xor_b32 s67, %[xcd], 5\n"
      "s_mul_i32 s67, s67, 32768\n"
      "s_add_u32 s67, s67, 2560\n"
      "v_add_u32 v16, s67, v10\n"
      "s_xor_b32 s67, %[xcd], 6\n"
      "s_mul_i32 s67, s67, 32768\n"
      "s_add_u32 s67, s67, 3072\n"
      "v_add_u32 v17, s67, v10\n"
      "s_xor_b32 s67, %[xcd], 7\n"
      "s_mul_i32 s67, s67, 32768\n"
      "s_add_u32 s67, s67, 3584\n"
      "v_add_u32 v18, s67, v10\n"
      "v_and_b32 v10, 15, %[ln]\n"
      "v_lshrrev_b32 v11, 4, %[ln]\n"
      "v_lshlrev_b32 v11, 3, v11\n"
      "v_lshl_add_u32 v19, v10, 11, v11\n"
      "s_mov_b32 s90, 0x3fff\n"
      "s_waitcnt vmcnt(12) lgkmcnt(0)\n"
      "v_max3_u32 v8, v64, v65, v66\n"
      "v_max3_u32 v8, v8, v67, v68\n"
      "v_max3_u32 v8, v8, v69, v70\n"
      "v_max_u32 v8, v8, v71\n"
      "v_cmp_eq_u32 vcc, -1, v8\n"
      "s_cbranch_vccnz .Ltf1_%=\n"
      "s_andn2_b32 s90, s90, 3\n"
      "v_mfma_f32_16x16x32_bf16 v[32:35], v[24:27], v[64:67], v[32:35]\n"
      "v_mfma_f32_16x16x32_bf16 v[36:39], v[28:31], v[64:67], v[36:39]\n"
      "ds_read_b64_tr_b16 v[24:25], v20 offset:28672\n"
      "ds_read_b64_tr_b16 v[26:27], v20 offset:29696\n"
      "v_mfma_f32_16x16x32_bf16 v[40:43], v[120:123], v[64:67], v[40:43]\n"
      "ds_read_b64_tr_b16 v[28:29], v21 offset:28672\n"
      "ds_read_b64_tr_b16 v[30:31], v21 offset:29696\n"
      "v_mfma_f32_16x16x32_bf16 v[44:47], v[124:127], v[64:67], v[44:47]\n"
      "ds_read_b64_tr_b16 v[120:121], v22 offset:28672\n"
      "ds_read_b64_tr_b16 v[122:123], v22 offset:29696\n"
      "s_waitcnt lgkmcnt(4)\n"
      "v_mfma_f32_16x16x32_bf16 v[48:51], v[24:27], v[64:67], v[48:51]\n"
      "ds_read_b64_tr_b16 v[124:125], v23 offset:28672\n"
      "ds_read_b64_tr_b16 v[126:127], v23 offset:29696\n"
      "s_waitcnt lgkmcnt(4)\n"
      "v_mfma_f32_16x16x32_bf16 v[52:55], v[28:31], v[64:67], v[52:55]\n"
      "ds_read_b64_tr_b16 v[24:25], v20 offset:16384\n"
      "ds_read_b64_tr_b16 v[26:27], v20 offset:17408\n"
      "s_waitcnt lgkmcnt(4)\n"
      "v_mfma_f32_16x16x32_bf16 v[56:59], v[120:123], v[64:67], v[56:59]\n"
      "ds_read_b64_tr_b16 v[28:29], v21 offset:16384\n"
      "ds_read_b64_tr_b16 v[30:31], v21 offset:17408\n"
      "s_waitcnt lgkmcnt(4)\n"
      "v_mfma_f32_16x16x32_bf16 v[60:63], v[124:127], v[64:67], v[60:63]\n"
      "ds_read_b64_tr_b16 v[120:121], v22 offset:16384\n"
      "ds_read_b64_tr_b16 v[122:123], v22 offset:17408\n"
      "s_waitcnt lgkmcnt(4)\n"
      "v_mfma_f32_16x16x32_bf16 v[32:35], v[24:27], v[68:71], v[32:35]\n"
      "ds_read_b64_tr_b16 v[124:125], v23 offset:16384\n"
      "ds_read_b64_tr_b16 v[126:127], v23 offset:17408\n"
      "s_waitcnt lgkmcnt(4)\n"
      "v_mfma_f32_16x16x32_bf16 v[36:39], v[28:31], v[68:71], v[36:39]\n"
      "ds_read_b64_tr_b16 v[24:25], v20 offset:20480\n"
      "ds_read_b64_tr_b16 v[26:27], v20 offset:21504\n"
      "s_waitcnt lgkmcnt(4)\n"
      "v_mfma_f32_16x16x32_bf16 v[40:43], v[120:123], v[68:71], v[40:43]\n"
      "ds_read_b64_tr_b16 v[28:29], v21 offset:20480\n"
      "ds_read_b64_tr_b16 v[30:31], v21 offset:21504\n"
      "s_waitcnt lgkmcnt(4)\n"
      "v_mfma_f32_16x16x32_bf16 v[44:47], v[124:127], v[68:71], v[44:47]\n"
      "ds_read_b64_tr_b16 v[120:121], v22 offset:20480\n"
      "ds_read_b64_tr_b16 v[122:123], v22 offset:21504\n"
      "s_waitcnt lgkmcnt(4)\n"
      "v_mfma_f32_16x16x32_bf16 v[48:51], v[24:27], v[68:71], v[48:51]\n"
      "ds_read_b64_tr_b16 v[124:125], v23 offset:20480\n"
      "ds_read_b64_tr_b16 v[126:127], v23 offset:21504\n"
      "s_waitcnt lgkmcnt(4)\n"
      "v_mfma_f32_16x16x32_bf16 v[52:55], v[28:31], v[68:71], v[52:55]\n"
      "s_waitcnt lgkmcnt(2)\n"
      "v_mfma_f32_16x16x32_bf16 v[56:59], v[120:123], v[68:71], v[56:59]\n"
      "s_waitcnt lgkmcnt(0)\n"
      "v_mfma_f32_16x16x32_bf16 v[60:63], v[124:127], v[68:71], v[60:63]\n"
      "s_waitcnt vmcnt(11)\n"
      "v_max3_u32 v8, v72, v73, v74\n"
      "v_max_u32 v8, v8, v75\n"
      "v_cmp_eq_u32 vcc, -1, v8\n"
      ".Ltn1_%=:\n"
      "s_cbranch_vccnz .Ltf2_%=\n"
      "s_andn2_b32 s90, s90, 4\n"
      "v_mfma_f32_16x16x32_bf16 v[32:35], a[0:3], v[72:75], v[32:35]\n"
      "v_mfma_f32_16x16x32_bf16 v[36:39], a[4:7], v[72:75], v[36:39]\n"
      "v_mfma_f32_16x16x32_bf16 v[40:43], a[8:11], v[72:75], v[40:43]\n"
      "v_mfma_f32_16x16x32_bf16 v[44:47], a[12:15], v[72:75], v[44:47]\n"
      "v_mfma_f32_16x16x32_bf16 v[48:51], a[16:19], v[72:75], v[48:51]\n"
      "s_waitcnt vmcnt(10)\n"
      "v_max3_u32 v8, v76, v77, v78\n"
      "v_max_u32 v8, v8, v79\n"
      "v_cmp_eq_u32 vcc, -1, v8\n"
      "v_mfma_f32_16x16x32_bf16 v[52:55], a[20:23], v[72:75], v[52:55]\n"
      "v_mfma_f32_16x16x32_bf16 v[56:59], a[24:27], v[72:75], v[56:59]\n"
      "v_mfma_f32_16x16x32_bf16 v[60:63], a[28:31], v[72:75], v[60:63]\n"
      ".Ltn2_%=:\n"
      "s_cbranch_vccnz .Ltf3_%=\n"
      "s_andn2_b32 s90, s90, 8\n"
      "v_mfma_f32_16x16x32_bf16 v[32:35], a[32:35], v[76:79], v[32:35]\n"
      "v_mfma_f32_16x16x32_bf16 v[36:39], a[36:39], v[76:79], v[36:39]\n"
      "v_mfma_f32_16x16x32_bf16 v[40:43], a[40:43], v[76:79], v[40:43]\n"
      "v_mfma_f32_16x16x32_bf16 v[44:47], a[44:47], v[76:79], v[44:47]\n"
      "v_mfma_f32_16x16x32_bf16 v[48:51], a[48:51], v[76:79], v[48:51]\n"
      "s_waitcnt vmcnt(9)\n"
      "v_max3_u32 v8, v80, v81, v82\n"
      "v_max_u32 v8, v8, v83\n"
      "v_cmp_eq_u32 vcc, -1, v8\n"
      "v_mfma_f32_16x16x32_bf16 v[52:55], a[52:55], v[76:79], v[52:55]\n"
      "v_mfma_f32_16x16x32_bf16 v[56:59], a[56:59], v[76:79], v[56:59]\n"
      "v_mfma_f32_16x16x32_bf16 v[60:63], a[60:63], v[76:79], v[60:63]\n"
      ".Ltn3_%=:\n"
      "s_cbranch_vccnz .Ltf4_%=\n"
      "s_andn2_b32 s90, s90, 16\n"
      "v_mfma_f32_16x16x32_bf16 v[32:35], a[64:67], v[80:83], v[32:35]\n"
      "v_mfma_f32_16x16x32_bf16 v[36:39], a[68:71], v[80:83], v[36:39]\n"
      "v_mfma_f32_16x16x32_bf16 v[40:43], a[72:75], v[80:83], v[40:43]\n"
      "v_mfma_f32_16x16x32_bf16 v[44:47], a[76:79], v[80:83], v[44:47]\n"
      "v_mfma_f32_16x16x32_bf16 v[48:51], a[80:83], v[80:83], v[48:51]\n"
      "s_waitcnt vmcnt(8)\n"
      "v_max3_u32 v8, v84, v85, v86\n"
      "v_max_u32 v8, v8, v87\n"
      "v_cmp_eq_u32 vcc, -1, v8\n"
      "v_mfma_f32_16x16x32_bf16 v[52:55], a[84:87], v[80:83], v[52:55]\n"
      "v_mfma_f32_16x16x32_bf16 v[56:59], a[88:91], v[80:83], v[56:59]\n"
      "v_mfma_f32_16x16x32_bf16 v[60:63], a[92:95], v[80:83], v[60:63]\n"
      ".Ltn4_%=:\n"
      "s_cbranch_vccnz .Ltf5_%=\n"
      "s_andn2_b32 s90, s90, 32\n"
      "v_mfma_f32_16x16x32_bf16 v[32:35], a[96:99], v[84:87], v[32:35]\n"
      "v_mfma_f32_16x16x32_bf16 v[36:39], a[100:103], v[84:87], v[36:39]\n"
      "v_mfma_f32_16x16x32_bf16 v[40:43], a[104:107], v[84:87], v[40:43]\n"
      "v_mfma_f32_16x16x32_bf16 v[44:47], a[108:111], v[84:87], v[44:47]\n"
      "v_mfma_f32_16x16x32_bf16 v[48:51], a[112:115], v[84:87], v[48:51]\n"
      "s_waitcnt vmcnt(7)\n"
      "v_max3_u32 v8, v88, v89, v90\n"
      "v_max_u32 v8, v8, v91\n"
      "v_cmp_eq_u32 vcc, -1, v8\n"
      "v_mfma_f32_16x16x32_bf16 v[52:55], a[116:119], v[84:87], v[52:55]\n"
      "v_mfma_f32_16x16x32_bf16 v[56:59], a[120:123], v[84:87], v[56:59]\n"
      "v_mfma_f32_16x16x32_bf16 v[60:63], a[124:127], v[84:87], v[60:63]\n"
      ".Ltn5_%=:\n"
      "s_cbranch_vccnz .Ltf6_%=\n"
      "s_andn2_b32 s90, s90, 64\n"
      "v_mfma_f32_16x16x32_bf16 v[32:35], a[128:131], v[88:91], v[32:35]\n"
      "v_mfma_f32_16x16x32_bf16 v[36:39], a[132:135], v[88:91], v[36:39]\n"
      "v_mfma_f32_16x16x32_bf16 v[40:43], a[136:139], v[88:91], v[40:43]\n"
      "v_mfma_f32_16x16x32_bf16 v[44:47], a[140:143], v[88:91], v[44:47]\n"
      "v_mfma_f32_16x16x32_bf16 v[48:51], a[144:147], v[88:91], v[48:51]\n"
      "s_waitcnt vmcnt(6)\n"
      "v_max3_u32 v8, v92, v93, v94\n"
      "v_max_u32 v8, v8, v95\n"
      "v_cmp_eq_u32 vcc, -1, v8\n"
      "v_mfma_f32_16x16x32_bf16 v[52:55], a[148:151], v[88:91], v[52:55]\n"
      "v_mfma_f32_16x16x32_bf16 v[56:59], a[152:155], v[88:91], v[56:59]\n"
      "v_mfma_f32_16x16x32_bf16 v[60:63], a[156:159], v[88:91], v[60:63]\n"
      ".Ltn6_%=:\n"
      "s_cbranch_vccnz .Ltf7_%=\n"
      "s_andn2_b32 s90, s90, 128\n"
      "v_mfma_f32_16x16x32_bf16 v[32:35], a[160:163], v[92:95], v[32:35]\n"
      "v_mfma_f32_16x16x32_bf16 v[36:39], a[164:167], v[92:95], v[36:39]\n"
      "v_mfma_f32_16x16x32_bf16 v[40:43], a[168:171], v[92:95], v[40:43]\n"
      "v_mfma_f32_16x16x32_bf16 v[44:47], a[172:175], v[92:95], v[44:47]\n"
      "v_mfma_f32_16x16x32_bf16 v[48:51], a[176:179], v[92:95], v[48:51]\n"
      "s_waitcnt vmcnt(5)\n"
      "v_max3_u32 v8, v96, v97, v98\n"
      "v_max_u32 v8, v8, v99\n"
      "v_cmp_eq_u32 vcc, -1, v8\n"
      "v_mfma_f32_16x16x32_bf16 v[52:55], a[180:183], v[92:95], v[52:55]\n"
      "v_mfma_f32_16x16x32_bf16 v[56:59], a[184:187], v[92:95], v[56:59]\n"
      "v_mfma_f32_16x16x32_bf16 v[60:63], a[188:191], v[92:95], v[60:63]\n"
      ".Ltn7_%=:\n"
      "s_cbranch_vccnz .Ltf8_%=\n"
      "s_andn2_b32 s90, s90, 256\n"
      "v_mfma_f32_16x16x32_bf16 v[32:35], a[192:195], v[96:99], v[32:35]\n"
      "v_mfma_f32_16x16x32_bf16 v[36:39], a[196:199], v[96:99], v[36:39]\n"
      "v_mfma_f32_16x16x32_bf16 v[40:43], a[200:203], v[96:99], v[40:43]\n"
      "v_mfma_f32_16x16x32_bf16 v[44:47], a[204:207], v[96:99], v[44:47]\n"
      "v_mfma_f32_16x16x32_bf16 v[48:51], a[208:211], v[96:99], v[48:51]\n"
      "s_waitcnt vmcnt(4)\n"
      "v_max3_u32 v8, v100, v101, v102\n"
      "v_max_u32 v8, v8, v103\n"
      "v_cmp_eq_u32 vcc, -1, v8\n"
      "v_mfma_f32_16x16x32_bf16 v[52:55], a[212:215], v[96:99], v[52:55]\n"
      "v_mfma_f32_16x16x32_bf16 v[56:59], a[216:219], v[96:99], v[56:59]\n"
      "v_mfma_f32_16x16x32_bf16 v[60:63], a[220:223], v[96:99], v[60:63]\n"
      ".Ltn8_%=:\n"
      "s_cbranch_vccnz .Ltf9_%=\n"
      "s_andn2_b32 s90, s90, 512\n"
      "v_mfma_f32_16x16x32_bf16 v[32:35], a[224:227], v[100:103], v[32:35]\n"
      "v_mfma_f32_16x16x32_bf16 v[36:39], a[228:231], v[100:103], v[36:39]\n"
      "v_mfma_f32_16x16x32_bf16 v[40:43], a[232:235], v[100:103], v[40:43]\n"
      "v_mfma_f32_16x16x32_bf16 v[44:47], a[236:239], v[100:103], v[44:47]\n"
      "v_mfma_f32_16x16x32_bf16 v[48:51], a[240:243], v[100:103], v[48:51]\n"
      "s_waitcnt vmcnt(3)\n"
      "v_max3_u32 v8, v104, v105, v106\n"
      "v_max_u32 v8, v8, v107\n"
      "v_cmp_eq_u32 vcc, -1, v8\n"
      "v_mfma_f32_16x16x32_bf16 v[52:55], a[244:247], v[100:103], v[52:55]\n"
      "v_mfma_f32_16x16x32_bf16 v[56:59], a[248:251], v[100:103], v[56:59]\n"
      "v_mfma_f32_16x16x32_bf16 v[60:63], a[252:255], v[100:103], v[60:63]\n"
      ".Ltn9_%=:\n"
      "s_cbranch_vccnz .Ltf10_%=\n"
      "s_andn2_b32 s90, s90, 1024\n"
      "v_mfma_f32_16x16x32_bf16 v[32:35], v[128:131], v[104:107], v[32:35]\n"
      "v_mfma_f32_16x16x32_bf16 v[36:39], v[132:135], v[104:107], v[36:39]\n"
      "v_mfma_f32_16x16x32_bf16 v[40:43], v[136:139], v[104:107], v[40:43]\n"
      "v_mfma_f32_16x16x32_bf16 v[44:47], v[140:143], v[104:107], v[44:47]\n"
      "v_mfma_f32_16x16x32_bf16 v[48:51], v[144:147], v[104:107], v[48:51]\n"
      "s_waitcnt vmcnt(2)\n"
      "v_max3_u32 v8, v108, v109, v110\n"
      "v_max_u32 v8, v8, v111\n"
      "v_cmp_eq_u32 vcc, -1, v8\n"
      "v_mfma_f32_16x16x32_bf16 v[52:55], v[148:151], v[104:107], v[52:55]\n"
      "v_mfma_f32_16x16x32_bf16 v[56:59], v[152:155], v[104:107], v[56:59]\n"
      "v_mfma_f32_16x16x32_bf16 v[60:63], v[156:159], v[104:107], v[60:63]\n"
      ".Ltn10_%=:\n"
      "s_cbranch_vccnz .Ltf11_%=\n"
      "s_andn2_b32 s90, s90, 2048\n"
      "v_mfma_f32_16x16x32_bf16 v[32:35], v[160:163], v[108:111], v[32:35]\n"
      "v_mfma_f32_16x16x32_bf16 v[36:39], v[164:167], v[108:111], v[36:39]\n"
      "v_mfma_f32_16x16x32_bf16 v[40:43], v[168:171], v[108:111], v[40:43]\n"
      "v_mfma_f32_16x16x32_bf16 v[44:47], v[172:175], v[108:111], v[44:47]\n"
      "v_mfma_f32_16x16x32_bf16 v[48:51], v[176:179], v[108:111], v[48:51]\n"
      "s_waitcnt vmcnt(1)\n"
      "v_max3_u32 v8, v112, v113, v114\n"
      "v_max_u32 v8, v8, v115\n"
      "v_cmp_eq_u32 vcc, -1, v8\n"
      "v_mfma_f32_16x16x32_bf16 v[52:55], v[180:183], v[108:111], v[52:55]\n"
      "v_mfma_f32_16x16x32_bf16 v[56:59], v[184:187], v[108:111], v[56:59]\n"
      "v_mfma_f32_16x16x32_bf16 v[60:63], v[188:191], v[108:111], v[60:63]\n"
      ".Ltn11_%=:\n"
      "s_cbranch_vccnz .Ltf12_%=\n"
      "s_andn2_b32 s90, s90, 4096\n"
      "v_mfma_f32_16x16x32_bf16 v[32:35], v[192:195], v[112:115], v[32:35]\n"
      "v_mfma_f32_16x16x32_bf16 v[36:39], v[196:199], v[112:115], v[36:39]\n"
      "v_mfma_f32_16x16x32_bf16 v[40:43], v[200:203], v[112:115], v[40:43]\n"
      "v_mfma_f32_16x16x32_bf16 v[44:47], v[204:207], v[112:115], v[44:47]\n"
      "v_mfma_f32_16x16x32_bf16 v[48:51], v[208:211], v[112:115], v[48:51]\n"
      "s_waitcnt vmcnt(0)\n"
      "v_max3_u32 v8, v116, v117, v118\n"
      "v_max_u32 v8, v8, v119\n"
      "v_cmp_eq_u32 vcc, -1, v8\n"
      "v_mfma_f32_16x16x32_bf16 v[52:55], v[212:215], v[112:115], v[52:55]\n"
      "v_mfma_f32_16x16x32_bf16 v[56:59], v[216:219], v[112:115], v[56:59]\n"
      "v_mfma_f32_16x16x32_bf16 v[60:63], v[220:223], v[112:115], v[60:63]\n"
      ".Ltn12_%=:\n"
      "s_cbranch_vccnz .Ltf13_%=\n"
      "s_andn2_b32 s90, s90, 8192\n"
      "v_mfma_f32_16x16x32_bf16 v[32:35], v[224:227], v[116:119], v[32:35]\n"
      "v_mfma_f32_16x16x32_bf16 v[36:39], v[228:231], v[116:119], v[36:39]\n"
      "v_mfma_f32_16x16x32_bf16 v[40:43], v[232:235], v[116:119], v[40:43]\n"
      "v_mfma_f32_16x16x32_bf16 v[44:47], v[236:239], v[116:119], v[44:47]\n"
      "v_mfma_f32_16x16x32_bf16 v[48:51], v[240:243], v[116:119], v[48:51]\n"
      "v_mfma_f32_16x16x32_bf16 v[52:55], v[244:247], v[116:119], v[52:55]\n"
      "v_mfma_f32_16x16x32_bf16 v[56:59], v[248:251], v[116:119], v[56:59]\n"
      "v_mfma_f32_16x16x32_bf16 v[60:63], v[252:255], v[116:119], v[60:63]\n"
      ".Ltn13_%=:\n"
      "s_cmp_eq_u32 s90, 0\n"
      "s_cbranch_scc1 .Lbdone_%=\n"
      "s_branch .Lround_%=\n"
      ".Ltf1_%=:\n"
      "global_load_dwordx4 v[64:67], v4, s[68:69] offset:0 sc1\n"
      "global_load_dwordx4 v[68:71], v4, s[68:69] offset:1024 sc1\n"
      "s_waitcnt vmcnt(11)\n"
      "v_max3_u32 v8, v72, v73, v74\n"
      "v_max_u32 v8, v8, v75\n"
      "v_cmp_eq_u32 vcc, -1, v8\n"
      "s_branch .Ltn1_%=\n"
      ".Ltf2_%=:\n"
      "global_load_dwordx4 v[72:75], v4, s[70:71] offset:0 sc1\n"
      "s_waitcnt vmcnt(10)\n"
      "v_max3_u32 v8, v76, v77, v78\n"
      "v_max_u32 v8, v8, v79\n"
      "v_cmp_eq_u32 vcc, -1, v8\n"
      "s_branch .Ltn2_%=\n"
      ".Ltf3_%=:\n"
      "global_load_dwordx4 v[76:79], v4, s[70:71] offset:1024 sc1\n"
      "s_waitcnt vmcnt(9)\n"
      "v_max3_u32 v8, v80, v81, v82\n"
      "v_max_u32 v8, v8, v83\n"
      "v_cmp_eq_u32 vcc, -1, v8\n"
      "s_branch .Ltn3_%=\n"
      ".Ltf4_%=:\n"
      "global_load_dwordx4 v[80:83], v4, s[72:73] offset:0 sc1\n"
      "s_waitcnt vmcnt(8)\n"
      "v_max3_u32 v8, v84, v85, v86\n"
      "v_max_u32 v8, v8, v87\n"
      "v_cmp_eq_u32 vcc, -1, v8\n"
      "s_branch .Ltn4_%=\n"
      ".Ltf5_%=:\n"
      "global_load_dwordx4 v[84:87], v4, s[72:73] offset:1024 sc1\n"
      "s_waitcnt vmcnt(7)\n"
      "v_max3_u32 v8, v88, v89, v90\n"
      "v_max_u32 v8, v8, v91\n"
      "v_cmp_eq_u32 vcc, -1, v8\n"
      "s_branch .Ltn5_%=\n"
      ".Ltf6_%=:\n"
      "global_load_dwordx4 v[88:91], v4, s[74:75] offset:0 sc1\n"
      "s_waitcnt vmcnt(6)\n"
      "v_max3_u32 v8, v92, v93, v94\n"
      "v_max_u32 v8, v8, v95\n"
      "v_cmp_eq_u32 vcc, -1, v8\n"
      "s_branch .Ltn6_%=\n"
      ".Ltf7_%=:\n"
      "global_load_dwordx4 v[92:95], v4, s[74:75] offset:1024 sc1\n"
      "s_waitcnt vmcnt(5)\n"
      "v_max3_u32 v8, v96, v97, v98\n"
      "v_max_u32 v8, v8, v99\n"
      "v_cmp_eq_u32 vcc, -1, v8\n"
      "s_branch .Ltn7_%=\n"
      ".Ltf8_%=:\n"
      "global_load_dwordx4 v[96:99], v4, s[76:77] offset:0 sc1\n"
      "s_waitcnt vmcnt(4)\n"
      "v_max3_u32 v8, v100, v101, v102\n"
      "v_max_u32 v8, v8, v103\n"
      "v_cmp_eq_u32 vcc, -1, v8\n"
      "s_branch .Ltn8_%=\n"
      ".Ltf9_%=:\n"
      "global_load_dwordx4 v[100:103], v4, s[76:77] offset:1024 sc1\n"
      "s_waitcnt vmcnt(3)\n"
      "v_max3_u32 v8, v104, v105, v106\n"
      "v_max_u32 v8, v8, v107\n"
      "v_cmp_eq_u32 vcc, -1, v8\n"
      "s_branch .Ltn9_%=\n"
      ".Ltf10_%=:\n"
      "global_load_dwordx4 v[104:107], v4, s[78:79] offset:0 sc1\n"
      "s_waitcnt vmcnt(2)\n"
      "v_max3_u32 v8, v108, v109, v110\n"
      "v_max_u32 v8, v8, v111\n"
      "v_cmp_eq_u32 vcc, -1, v8\n"
      "s_branch .Ltn10_%=\n"
      ".Ltf11_%=:\n"
      "global_load_dwordx4 v[108:111], v4, s[78:79] offset:1024 sc1\n"
      "s_waitcnt vmcnt(1)\n"
      "v_max3_u32 v8, v112, v113, v114\n"
      "v_max_u32 v8, v8, v115\n"
      "v_cmp_eq_u32 vcc, -1, v8\n"
      "s_branch .Ltn11_%=\n"
      ".Ltf12_%=:\n"
      "global_load_dwordx4 v[112:115], v4, s[80:81] offset:0 sc1\n"
      "s_waitcnt vmcnt(0)\n"
      "v_max3_u32 v8, v116, v117, v118\n"
      "v_max_u32 v8, v8, v119\n"
      "v_cmp_eq_u32 vcc, -1, v8\n"
      "s_branch .Ltn12_%=\n"
      ".Ltf13_%=:\n"
      "global_load_dwordx4 v[116:119], v4, s[80:81] offset:1024 sc1\n"
      "s_branch .Ltn13_%=\n"
      ".Lgen_%=:\n"
      "global_load_dwordx2 v[6:7], v5, %[ws] sc1\n"
      "s_bitcmp1_b32 s90, 1\n"
      "s_cbranch_scc1 .Lir1_%=\n"
      "global_load_dword v9, v4, %[xq0]\n"
      "global_load_dword v9, v4, %[xq0]\n"
      "s_branch .Lin1_%=\n"
      ".Lir1_%=:\n"
      "global_load_dwordx4 v[64:67], v4, s[68:69] offset:0 sc1\n"
      "global_load_dwordx4 v[68:71], v4, s[68:69] offset:1024 sc1\n"
      ".Lin1_%=:\n"
      "s_bitcmp1_b32 s90, 2\n"
      "s_cbranch_scc1 .Lir2_%=\n"
      "global_load_dword v9, v4, %[xq0]\n"
      "s_branch .Lin2_%=\n"
      ".Lir2_%=:\n"
      "global_load_dwordx4 v[72:75], v4, s[70:71] offset:0 sc1\n"
      ".Lin2_%=:\n"
      "s_bitcmp1_b32 s90, 3\n"
      "s_cbranch_scc1 .Lir3_%=\n"
      "global_load_dword v9, v4, %[xq0]\n"
      "s_branch .Lin3_%=\n"
      ".Lir3_%=:\n"
      "global_load_dwordx4 v[76:79], v4, s[70:71] offset:1024 sc1\n"
      ".Lin3_%=:\n"
      "s_bitcmp1_b32 s90, 4\n"
      "s_cbranch_scc1 .Lir4_%=\n"
      "global_load_dword v9, v4, %[xq0]\n"
      "s_branch .Lin4_%=\n"
      ".Lir4_%=:\n"
      "global_load_dwordx4 v[80:83], v4, s[72:73] offset:0 sc1\n"
      ".Lin4_%=:\n"
      "s_bitcmp1_b32 s90, 5\n"
      "s_cbranch_scc1 .Lir5_%=\n"
      "global_load_dword v9, v4, %[xq0]\n"
      "s_branch .Lin5_%=\n"
      ".Lir5_%=:\n"
      "global_load_dwordx4 v[84:87], v4, s[72:73] offset:1024 sc1\n"
      ".Lin5_%=:\n"
      "s_bitcmp1_b32 s90, 6\n"
      "s_cbranch_scc1 .Lir6_%=\n"
      "global_load_dword v9, v4, %[xq0]\n"
      "s_branch .Lin6_%=\n"
      ".Lir6_%=:\n"
      "global_load_dwordx4 v[88:91], v4, s[74:75] offset:0 sc1\n"
      ".Lin6_%=:\n"
      "s_bitcmp1_b32 s90, 7\n"
      "s_cbranch_scc1 .Lir7_%=\n"
      "global_load_dword v9, v4, %[xq0]\n"
      "s_branch .Lin7_%=\n"
      ".Lir7_%=:\n"
      "global_load_dwordx4 v[92:95], v4, s[74:75] offset:1024 sc1\n"
      ".Lin7_%=:\n"
      "s_bitcmp1_b32 s90, 8\n"
      "s_cbranch_scc1 .Lir8_%=\n"
      "global_load_dword v9, v4, %[xq0]\n"
      "s_branch .Lin8_%=\n"
      ".Lir8_%=:\n"
      "global_load_dwordx4 v[96:99], v4, s[76:77] offset:0 sc1\n"
      ".Lin8_%=:\n"
      "s_bitcmp1_b32 s90, 9\n"
      "s_cbranch_scc1 .Lir9_%=\n"
      "global_load_dword v9, v4, %[xq0]\n"
      "s_branch .Lin9_%=\n"
      ".Lir9_%=:\n"
      "global_load_dwordx4 v[100:103], v4, s[76:77] offset:1024 sc1\n"
      ".Lin9_%=:\n"
      "s_bitcmp1_b32 s90, 10\n"
      "s_cbranch_scc1 .Lir10_%=\n"
      "global_load_dword v9, v4, %[xq0]\n"
      "s_branch .Lin10_%=\n"
      ".Lir10_%=:\n"
      "global_load_dwordx4 v[104:107], v4, s[78:79] offset:0 sc1\n"
      ".Lin10_%=:\n"
      "s_bitcmp1_b32 s90, 11\n"
      "s_cbranch_scc1 .Lir11_%=\n"
      "global_load_dword v9, v4, %[xq0]\n"
      "s_branch .Lin11_%=\n"
      ".Lir11_%=:\n"
      "global_load_dwordx4 v[108:111], v4, s[78:79] offset:1024 sc1\n"
      ".Lin11_%=:\n"
      "s_bitcmp1_b32 s90, 12\n"
      "s_cbranch_scc1 .Lir12_%=\n"
      "global_load_dword v9, v4, %[xq0]\n"
      "s_branch .Lin12_%=\n"
      ".Lir12_%=:\n"
      "global_load_dwordx4 v[112:115], v4, s[80:81] offset:0 sc1\n"
      ".Lin12_%=:\n"
      "s_bitcmp1_b32 s90, 13\n"
      "s_cbranch_scc1 .Lir13_%=\n"
      "global_load_dword v9, v4, %[xq0]\n"
      "s_branch .Lin13_%=\n"
      ".Lir13_%=:\n"
      "global_load_dwordx4 v[116:119], v4, s[80:81] offset:1024 sc1\n"
      ".Lin13_%=:\n"
      "s_waitcnt lgkmcnt(0)\n"
      ".Lround_%=:\n"
      "s_add_u32 s96, s96, 1\n"
      "s_waitcnt vmcnt(14)\n"
      "global_load_dwordx2 v[6:7], v5, %[ws] sc1\n"
      "s_waitcnt vmcnt(13)\n"
      "s_bitcmp1_b32 s90, 1\n"
      "s_cbranch_scc0 .Lgd1_%=\n"
      "v_max3_u32 v8, v64, v65, v66\n"
      "v_max3_u32 v8, v8, v67, v68\n"
      "v_max3_u32 v8, v8, v69, v70\n"
      "v_max_u32 v8, v8, v71\n"
      "v_cmp_eq_u32 vcc, -1, v8\n"
      "s_cmp_lt_u32 s96, 4194304\n"
      "s_cselect_b64 vcc, vcc, 0\n"
      "s_cbranch_vccnz .Lgr1_%=\n"
      "s_andn2_b32 s90, s90, 3\n"
      "v_mfma_f32_16x16x32_bf16 v[32:35], v[24:27], v[64:67], v[32:35]\n"
      "v_mfma_f32_16x16x32_bf16 v[36:39], v[28:31], v[64:67], v[36:39]\n"
      "ds_read_b64_tr_b16 v[24:25], v20 offset:28672\n"
      "ds_read_b64_tr_b16 v[26:27], v20 offset:29696\n"
      "v_mfma_f32_16x16x32_bf16 v[40:43], v[120:123], v[64:67], v[40:43]\n"
      "ds_read_b64_tr_b16 v[28:29], v21 offset:28672\n"
      "ds_read_b64_tr_b16 v[30:31], v21 offset:29696\n"
      "v_mfma_f32_16x16x32_bf16 v[44:47], v[124:127], v[64:67], v[44:47]\n"
      "ds_read_b64_tr_b16 v[120:121], v22 offset:28672\n"
      "ds_read_b64_tr_b16 v[122:123], v22 offset:29696\n"
      "s_waitcnt lgkmcnt(4)\n"
      "v_mfma_f32_16x16x32_bf16 v[48:51], v[24:27], v[64:67], v[48:51]\n"
      "ds_read_b64_tr_b16 v[124:125], v23 offset:28672\n"
      "ds_read_b64_tr_b16 v[126:127], v23 offset:29696\n"
      "s_waitcnt lgkmcnt(4)\n"
      "v_mfma_f32_16x16x32_bf16 v[52:55], v[28:31], v[64:67], v[52:55]\n"
      "ds_read_b64_tr_b16 v[24:25], v20 offset:16384\n"
      "ds_read_b64_tr_b16 v[26:27], v20 offset:17408\n"
      "s_waitcnt lgkmcnt(4)\n"
      "v_mfma_f32_16x16x32_bf16 v[56:59], v[120:123], v[64:67], v[56:59]\n"
      "ds_read_b64_tr_b16 v[28:29], v21 offset:16384\n"
      "ds_read_b64_tr_b16 v[30:31], v21 offset:17408\n"
      "s_waitcnt lgkmcnt(4)\n"
      "v_mfma_f32_16x16x32_bf16 v[60:63], v[124:127], v[64:67], v[60:63]\n"
      "ds_read_b64_tr_b16 v[120:121], v22 offset:16384\n"
      "ds_read_b64_tr_b16 v[122:123], v22 offset:17408\n"
      "s_waitcnt lgkmcnt(4)\n"
      "v_mfma_f32_16x16x32_bf16 v[32:35], v[24:27], v[68:71], v[32:35]\n"
      "ds_read_b64_tr_b16 v[124:125], v23 offset:16384\n"
      "ds_read_b64_tr_b16 v[126:127], v23 offset:17408\n"
      "s_waitcnt lgkmcnt(4)\n"
      "v_mfma_f32_16x16x32_bf16 v[36:39], v[28:31], v[68:71], v[36:39]\n"
      "ds_read_b64_tr_b16 v[24:25], v20 offset:20480\n"
      "ds_read_b64_tr_b16 v[26:27], v20 offset:21504\n"
      "s_waitcnt lgkmcnt(4)\n"
      "v_mfma_f32_16x16x32_bf16 v[40:43], v[120:123], v[68:71], v[40:43]\n"
      "ds_read_b64_tr_b16 v[28:29], v21 offset:20480\n"
      "ds_read_b64_tr_b16 v[30:31], v21 offset:21504\n"
      "s_waitcnt lgkmcnt(4)\n"
      "v_mfma_f32_16x16x32_bf16 v[44:47], v[124:127], v[68:71], v[44:47]\n"
      "ds_read_b64_tr_b16 v[120:121], v22 offset:20480\n"
      "ds_read_b64_tr_b16 v[122:123], v22 offset:21504\n"
      "s_waitcnt lgkmcnt(4)\n"
      "v_mfma_f32_16x16x32_bf16 v[48:51], v[24:27], v[68:71], v[48:51]\n"
      "ds_read_b64_tr_b16 v[124:125], v23 offset:20480\n"
      "ds_read_b64_tr_b16 v[126:127], v23 offset:21504\n"
      "s_waitcnt lgkmcnt(4)\n"
      "v_mfma_f32_16x16x32_bf16 v[52:55], v[28:31], v[68:71], v[52:55]\n"
      "s_waitcnt lgkmcnt(2)\n"
      "v_mfma_f32_16x16x32_bf16 v[56:59], v[120:123], v[68:71], v[56:59]\n"
      "s_waitcnt lgkmcnt(0)\n"
      "v_mfma_f32_16x16x32_bf16 v[60:63], v[124:127], v[68:71], v[60:63]\n"
      "s_cmp_eq_u32 s90, 0\n"
      "s_cbranch_scc1 .Lbdone_%=\n"
      ".Lgd1_%=:\n"
      "global_load_dword v9, v4, %[xq0]\n"
      "global_load_dword v9, v4, %[xq0]\n"
      "s_branch .Lgn1_%=\n"
      ".Lgr1_%=:\n"
      "global_load_dwordx4 v[64:67], v4, s[68:69] offset:0 sc1\n"
      "global_load_dwordx4 v[68:71], v4, s[68:69] offset:1024 sc1\n"
      ".Lgn1_%=:\n"
      "s_waitcnt vmcnt(14)\n"
      "s_bitcmp1_b32 s90, 2\n"
      "s_cbranch_scc0 .Lgd2_%=\n"
      "v_max3_u32 v8, v72, v73, v74\n"
      "v_max_u32 v8, v8, v75\n"
      "v_cmp_eq_u32 vcc, -1, v8\n"
      "s_cmp_lt_u32 s96, 4194304\n"
      "s_cselect_b64 vcc, vcc, 0\n"
      "s_cbranch_vccnz .Lgr2_%=\n"
      "s_andn2_b32 s90, s90, 4\n"
      "v_mfma_f32_16x16x32_bf16 v[32:35], a[0:3], v[72:75], v[32:35]\n"
      "v_mfma_f32_16x16x32_bf16 v[36:39], a[4:7], v[72:75], v[36:39]\n"
      "v_mfma_f32_16x16x32_bf16 v[40:43], a[8:11], v[72:75], v[40:43]\n"
      "v_mfma_f32_16x16x32_bf16 v[44:47], a[12:15], v[72:75], v[44:47]\n"
      "v_mfma_f32_16x16x32_bf16 v[48:51], a[16:19], v[72:75], v[48:51]\n"
      "v_mfma_f32_16x16x32_bf16 v[52:55], a[20:23], v[72:75], v[52:55]\n"
      "v_mfma_f32_16x16x32_bf16 v[56:59], a[24:27], v[72:75], v[56:59]\n"
      "v_mfma_f32_16x16x32_bf16 v[60:63], a[28:31], v[72:75], v[60:63]\n"
      "s_cmp_eq_u32 s90, 0\n"
      "s_cbranch_scc1 .Lbdone_%=\n"
      ".Lgd2_%=:\n"
      "global_load_dword v9, v4, %[xq0]\n"
      "s_branch .Lgn2_%=\n"
      ".Lgr2_%=:\n"
      "global_load_dwordx4 v[72:75], v4, s[70:71] offset:0 sc1\n"
      ".Lgn2_%=:\n"
      "s_waitcnt vmcnt(14)\n"
      "s_bitcmp1_b32 s90, 3\n"
      "s_cbranch_scc0 .Lgd3_%=\n"
      "v_max3_u32 v8, v76, v77, v78\n"
      "v_max_u32 v8, v8, v79\n"
      "v_cmp_eq_u32 vcc, -1, v8\n"
      "s_cmp_lt_u32 s96, 4194304\n"
      "s_cselect_b64 vcc, vcc, 0\n"
      "s_cbranch_vccnz .Lgr3_%=\n"
      "s_andn2_b32 s90, s90, 8\n"
      "v_mfma_f32_16x16x32_bf16 v[32:35], a[32:35], v[76:79], v[32:35]\n"
      "v_mfma_f32_16x16x32_bf16 v[36:39], a[36:39], v[76:79], v[36:39]\n"
      "v_mfma_f32_16x16x32_bf16 v[40:43], a[40:43], v[76:79], v[40:43]\n"
      "v_mfma_f32_16x16x32_bf16 v[44:47], a[44:47], v[76:79], v[44:47]\n"
      "v_mfma_f32_16x16x32_bf16 v[48:51], a[48:51], v[76:79], v[48:51]\n"
      "v_mfma_f32_16x16x32_bf16 v[52:55], a[52:55], v[76:79], v[52:55]\n"
      "v_mfma_f32_16x16x32_bf16 v[56:59], a[56:59], v[76:79], v[56:59]\n"
      "v_mfma_f32_16x16x32_bf16 v[60:63], a[60:63], v[76:79], v[60:63]\n"
      "s_cmp_eq_u32 s90, 0\n"
      "s_cbranch_scc1 .Lbdone_%=\n"
      ".Lgd3_%=:\n"
      "global_load_dword v9, v4, %[xq0]\n"
      "s_branch .Lgn3_%=\n"
      ".Lgr3_%=:\n"
      "global_load_dwordx4 v[76:79], v4, s[70:71] offset:1024 sc1\n"
      ".Lgn3_%=:\n"
      "s_waitcnt vmcnt(14)\n"
      "s_bitcmp1_b32 s90, 4\n"
      "s_cbranch_scc0 .Lgd4_%=\n"
      "v_max3_u32 v8, v80, v81, v82\n"
      "v_max_u32 v8, v8, v83\n"
      "v_cmp_eq_u32 vcc, -1, v8\n"
      "s_cmp_lt_u32 s96, 4194304\n"
      "s_cselect_b64 vcc, vcc, 0\n"
      "s_cbranch_vccnz .Lgr4_%=\n"
      "s_andn2_b32 s90, s90, 16\n"
      "v_mfma_f32_16x16x32_bf16 v[32:35], a[64:67], v[80:83], v[32:35]\n"
      "v_mfma_f32_16x16x32_bf16 v[36:39], a[68:71], v[80:83], v[36:39]\n"
      "v_mfma_f32_16x16x32_bf16 v[40:43], a[72:75], v[80:83], v[40:43]\n"
      "v_mfma_f32_16x16x32_bf16 v[44:47], a[76:79], v[80:83], v[44:47]\n"
      "v_mfma_f32_16x16x32_bf16 v[48:51], a[80:83], v[80:83], v[48:51]\n"
      "v_mfma_f32_16x16x32_bf16 v[52:55], a[84:87], v[80:83], v[52:55]\n"
      "v_mfma_f32_16x16x32_bf16 v[56:59], a[88:91], v[80:83], v[56:59]\n"
      "v_mfma_f32_16x16x32_bf16 v[60:63], a[92:95], v[80:83], v[60:63]\n"
      "s_cmp_eq_u32 s90, 0\n"
      "s_cbranch_scc1 .Lbdone_%=\n"
      ".Lgd4_%=:\n"
      "global_load_dword v9, v4, %[xq0]\n"
      "s_branch .Lgn4_%=\n"
      ".Lgr4_%=:\n"
      "global_load_dwordx4 v[80:83], v4, s[72:73] offset:0 sc1\n"
      ".Lgn4_%=:\n"
      "s_waitcnt vmcnt(14)\n"
      "s_bitcmp1_b32 s90, 5\n"
      "s_cbranch_scc0 .Lgd5_%=\n"
      "v_max3_u32 v8, v84, v85, v86\n"
      "v_max_u32 v8, v8, v87\n"
      "v_cmp_eq_u32 vcc, -1, v8\n"
      "s_cmp_lt_u32 s96, 4194304\n"
      "s_cselect_b64 vcc, vcc, 0\n"
      "s_cbranch_vccnz .Lgr5_%=\n"
      "s_andn2_b32 s90, s90, 32\n"
      "v_mfma_f32_16x16x32_bf16 v[32:35], a[96:99], v[84:87], v[32:35]\n"
      "v_mfma_f32_16x16x32_bf16 v[36:39], a[100:103], v[84:87], v[36:39]\n"
      "v_mfma_f32_16x16x32_bf16 v[40:43], a[104:107], v[84:87], v[40:43]\n"
      "v_mfma_f32_16x16x32_bf16 v[44:47], a[108:111], v[84:87], v[44:47]\n"
      "v_mfma_f32_16x16x32_bf16 v[48:51], a[112:115], v[84:87], v[48:51]\n"
      "v_mfma_f32_16x16x32_bf16 v[52:55], a[116:119], v[84:87], v[52:55]\n"
      "v_mfma_f32_16x16x32_bf16 v[56:59], a[120:123], v[84:87], v[56:59]\n"
      "v_mfma_f32_16x16x32_bf16 v[60:63], a[124:127], v[84:87], v[60:63]\n"
      "s_cmp_eq_u32 s90, 0\n"
      "s_cbranch_scc1 .Lbdone_%=\n"
      ".Lgd5_%=:\n"
      "global_load_dword v9, v4, %[xq0]\n"
      "s_branch .Lgn5_%=\n"
      ".Lgr5_%=:\n"
      "global_load_dwordx4 v[84:87], v4, s[72:73] offset:1024 sc1\n"
      ".Lgn5_%=:\n"
      "s_waitcnt vmcnt(14)\n"
      "s_bitcmp1_b32 s90, 6\n"
      "s_cbranch_scc0 .Lgd6_%=\n"
      "v_max3_u32 v8, v88, v89, v90\n"
      "v_max_u32 v8, v8, v91\n"
      "v_cmp_eq_u32 vcc, -1, v8\n"
      "s_cmp_lt_u32 s96, 4194304\n"
      "s_cselect_b64 vcc, vcc, 0\n"
      "s_cbranch_vccnz .Lgr6_%=\n"
      "s_andn2_b32 s90, s90, 64\n"
      "v_mfma_f32_16x16x32_bf16 v[32:35], a[128:131], v[88:91], v[32:35]\n"
      "v_mfma_f32_16x16x32_bf16 v[36:39], a[132:135], v[88:91], v[36:39]\n"
      "v_mfma_f32_16x16x32_bf16 v[40:43], a[136:139], v[88:91], v[40:43]\n"
      "v_mfma_f32_16x16x32_bf16 v[44:47], a[140:143], v[88:91], v[44:47]\n"
      "v_mfma_f32_16x16x32_bf16 v[48:51], a[144:147], v[88:91], v[48:51]\n"
      "v_mfma_f32_16x16x32_bf16 v[52:55], a[148:151], v[88:91], v[52:55]\n"
      "v_mfma_f32_16x16x32_bf16 v[56:59], a[152:155], v[88:91], v[56:59]\n"
      "v_mfma_f32_16x16x32_bf16 v[60:63], a[156:159], v[88:91], v[60:63]\n"
      "s_cmp_eq_u32 s90, 0\n"
      "s_cbranch_scc1 .Lbdone_%=\n"
      ".Lgd6_%=:\n"
      "global_load_dword v9, v4, %[xq0]\n"
      "s_branch .Lgn6_%=\n"
      ".Lgr6_%=:\n"
      "global_load_dwordx4 v[88:91], v4, s[74:75] offset:0 sc1\n"
      ".Lgn6_%=:\n"
      "s_waitcnt vmcnt(14)\n"
      "s_bitcmp1_b32 s90, 7\n"
      "s_cbranch_scc0 .Lgd7_%=\n"
      "v_max3_u32 v8, v92, v93, v94\n"
      "v_max_u32 v8, v8, v95\n"
      "v_cmp_eq_u32 vcc, -1, v8\n"
      "s_cmp_lt_u32 s96, 4194304\n"
      "s_cselect_b64 vcc, vcc, 0\n"
      "s_cbranch_vccnz .Lgr7_%=\n"
      "s_andn2_b32 s90, s90, 128\n"
      "v_mfma_f32_16x16x32_bf16 v[32:35], a[160:163], v[92:95], v[32:35]\n"
      "v_mfma_f32_16x16x32_bf16 v[36:39], a[164:167], v[92:95], v[36:39]\n"
      "v_mfma_f32_16x16x32_bf16 v[40:43], a[168:171], v[92:95], v[40:43]\n"
      "v_mfma_f32_16x16x32_bf16 v[44:47], a[172:175], v[92:95], v[44:47]\n"
      "v_mfma_f32_16x16x32_bf16 v[48:51], a[176:179], v[92:95], v[48:51]\n"
      "v_mfma_f32_16x16x32_bf16 v[52:55], a[180:183], v[92:95], v[52:55]\n"
      "v_mfma_f32_16x16x32_bf16 v[56:59], a[184:187], v[92:95], v[56:59]\n"
      "v_mfma_f32_16x16x32_bf16 v[60:63], a[188:191], v[92:95], v[60:63]\n"
      "s_cmp_eq_u32 s90, 0\n"
      "s_cbranch_scc1 .Lbdone_%=\n"
      ".Lgd7_%=:\n"
      "global_load_dword v9, v4, %[xq0]\n"
      "s_branch .Lgn7_%=\n"
      ".Lgr7_%=:\n"
      "global_load_dwordx4 v[92:95], v4, s[74:75] offset:1024 sc1\n"
      ".Lgn7_%=:\n"
      "s_waitcnt vmcnt(14)\n"
      "s_bitcmp1_b32 s90, 8\n"
      "s_cbranch_scc0 .Lgd8_%=\n"
      "v_max3_u32 v8, v96, v97, v98\n"
      "v_max_u32 v8, v8, v99\n"
      "v_cmp_eq_u32 vcc, -1, v8\n"
      "s_cmp_lt_u32 s96, 4194304\n"
      "s_cselect_b64 vcc, vcc, 0\n"
      "s_cbranch_vccnz .Lgr8_%=\n"
      "s_andn2_b32 s90, s90, 256\n"
      "v_mfma_f32_16x16x32_bf16 v[32:35], a[192:195], v[96:99], v[32:35]\n"
      "v_mfma_f32_16x16x32_bf16 v[36:39], a[196:199], v[96:99], v[36:39]\n"
      "v_mfma_f32_16x16x32_bf16 v[40:43], a[200:203], v[96:99], v[40:43]\n"
      "v_mfma_f32_16x16x32_bf16 v[44:47], a[204:207], v[96:99], v[44:47]\n"
      "v_mfma_f32_16x16x32_bf16 v[48:51], a[208:211], v[96:99], v[48:51]\n"
      "v_mfma_f32_16x16x32_bf16 v[52:55], a[212:215], v[96:99], v[52:55]\n"
      "v_mfma_f32_16x16x32_bf16 v[56:59], a[216:219], v[96:99], v[56:59]\n"
      "v_mfma_f32_16x16x32_bf16 v[60:63], a[220:223], v[96:99], v[60:63]\n"
      "s_cmp_eq_u32 s90, 0\n"
      "s_cbranch_scc1 .Lbdone_%=\n"
      ".Lgd8_%=:\n"
      "global_load_dword v9, v4, %[xq0]\n"
      "s_branch .Lgn8_%=\n"
      ".Lgr8_%=:\n"
      "global_load_dwordx4 v[96:99], v4, s[76:77] offset:0 sc1\n"
      ".Lgn8_%=:\n"
      "s_waitcnt vmcnt(14)\n"
      "s_bitcmp1_b32 s90, 9\n"
      "s_cbranch_scc0 .Lgd9_%=\n"
      "v_max3_u32 v8, v100, v101, v102\n"
      "v_max_u32 v8, v8, v103\n"
      "v_cmp_eq_u32 vcc, -1, v8\n"
      "s_cmp_lt_u32 s96, 4194304\n"
      "s_cselect_b64 vcc, vcc, 0\n"
      "s_cbranch_vccnz .Lgr9_%=\n"
      "s_andn2_b32 s90, s90, 512\n"
      "v_mfma_f32_16x16x32_bf16 v[32:35], a[224:227], v[100:103], v[32:35]\n"
      "v_mfma_f32_16x16x32_bf16 v[36:39], a[228:231], v[100:103], v[36:39]\n"
      "v_mfma_f32_16x16x32_bf16 v[40:43], a[232:235], v[100:103], v[40:43]\n"
      "v_mfma_f32_16x16x32_bf16 v[44:47], a[236:239], v[100:103], v[44:47]\n"
      "v_mfma_f32_16x16x32_bf16 v[48:51], a[240:243], v[100:103], v[48:51]\n"
      "v_mfma_f32_16x16x32_bf16 v[52:55], a[244:247], v[100:103], v[52:55]\n"
      "v_mfma_f32_16x16x32_bf16 v[56:59], a[248:251], v[100:103], v[56:59]\n"
      "v_mfma_f32_16x16x32_bf16 v[60:63], a[252:255], v[100:103], v[60:63]\n"
      "s_cmp_eq_u32 s90, 0\n"
      "s_cbranch_scc1 .Lbdone_%=\n"
      ".Lgd9_%=:\n"
      "global_load_dword v9, v4, %[xq0]\n"
      "s_branch .Lgn9_%=\n"
      ".Lgr9_%=:\n"
      "global_load_dwordx4 v[100:103], v4, s[76:77] offset:1024 sc1\n"
      ".Lgn9_%=:\n"
      "s_waitcnt vmcnt(14)\n"
      "s_bitcmp1_b32 s90, 10\n"
      "s_cbranch_scc0 .Lgd10_%=\n"
      "v_max3_u32 v8, v104, v105, v106\n"
      "v_max_u32 v8, v8, v107\n"
      "v_cmp_eq_u32 vcc, -1, v8\n"
      "s_cmp_lt_u32 s96, 4194304\n"
      "s_cselect_b64 vcc, vcc, 0\n"
      "s_cbranch_vccnz .Lgr10_%=\n"
      "s_andn2_b32 s90, s90, 1024\n"
      "v_mfma_f32_16x16x32_bf16 v[32:35], v[128:131], v[104:107], v[32:35]\n"
      "v_mfma_f32_16x16x32_bf16 v[36:39], v[132:135], v[104:107], v[36:39]\n"
      "v_mfma_f32_16x16x32_bf16 v[40:43], v[136:139], v[104:107], v[40:43]\n"
      "v_mfma_f32_16x16x32_bf16 v[44:47], v[140:143], v[104:107], v[44:47]\n"
      "v_mfma_f32_16x16x32_bf16 v[48:51], v[144:147], v[104:107], v[48:51]\n"
      "v_mfma_f32_16x16x32_bf16 v[52:55], v[148:151], v[104:107], v[52:55]\n"
      "v_mfma_f32_16x16x32_bf16 v[56:59], v[152:155], v[104:107], v[56:59]\n"
      "v_mfma_f32_16x16x32_bf16 v[60:63], v[156:159], v[104:107], v[60:63]\n"
      "s_cmp_eq_u32 s90, 0\n"
      "s_cbranch_scc1 .Lbdone_%=\n"
      ".Lgd10_%=:\n"
      "global_load_dword v9, v4, %[xq0]\n"
      "s_branch .Lgn10_%=\n"
      ".Lgr10_%=:\n"
      "global_load_dwordx4 v[104:107], v4, s[78:79] offset:0 sc1\n"
      ".Lgn10_%=:\n"
      "s_waitcnt vmcnt(14)\n"
      "s_bitcmp1_b32 s90, 11\n"
      "s_cbranch_scc0 .Lgd11_%=\n"
      "v_max3_u32 v8, v108, v109, v110\n"
      "v_max_u32 v8, v8, v111\n"
      "v_cmp_eq_u32 vcc, -1, v8\n"
      "s_cmp_lt_u32 s96, 4194304\n"
      "s_cselect_b64 vcc, vcc, 0\n"
      "s_cbranch_vccnz .Lgr11_%=\n"
      "s_andn2_b32 s90, s90, 2048\n"
      "v_mfma_f32_16x16x32_bf16 v[32:35], v[160:163], v[108:111], v[32:35]\n"
      "v_mfma_f32_16x16x32_bf16 v[36:39], v[164:167], v[108:111], v[36:39]\n"
      "v_mfma_f32_16x16x32_bf16 v[40:43], v[168:171], v[108:111], v[40:43]\n"
      "v_mfma_f32_16x16x32_bf16 v[44:47], v[172:175], v[108:111], v[44:47]\n"
      "v_mfma_f32_16x16x32_bf16 v[48:51], v[176:179], v[108:111], v[48:51]\n"
      "v_mfma_f32_16x16x32_bf16 v[52:55], v[180:183], v[108:111], v[52:55]\n"
      "v_mfma_f32_16x16x32_bf16 v[56:59], v[184:187], v[108:111], v[56:59]\n"
      "v_mfma_f32_16x16x32_bf16 v[60:63], v[188:191], v[108:111], v[60:63]\n"
      "s_cmp_eq_u32 s90, 0\n"
      "s_cbranch_scc1 .Lbdone_%=\n"
      ".Lgd11_%=:\n"
      "global_load_dword v9, v4, %[xq0]\n"
      "s_branch .Lgn11_%=\n"
      ".Lgr11_%=:\n"
      "global_load_dwordx4 v[108:111], v4, s[78:79] offset:1024 sc1\n"
      ".Lgn11_%=:\n"
      "s_waitcnt vmcnt(14)\n"
      "s_bitcmp1_b32 s90, 12\n"
      "s_cbranch_scc0 .Lgd12_%=\n"
      "v_max3_u32 v8, v112, v113, v114\n"
      "v_max_u32 v8, v8, v115\n"
      "v_cmp_eq_u32 vcc, -1, v8\n"
      "s_cmp_lt_u32 s96, 4194304\n"
      "s_cselect_b64 vcc, vcc, 0\n"
      "s_cbranch_vccnz .Lgr12_%=\n"
      "s_andn2_b32 s90, s90, 4096\n"
      "v_mfma_f32_16x16x32_bf16 v[32:35], v[192:195], v[112:115], v[32:35]\n"
      "v_mfma_f32_16x16x32_bf16 v[36:39], v[196:199], v[112:115], v[36:39]\n"
      "v_mfma_f32_16x16x32_bf16 v[40:43], v[200:203], v[112:115], v[40:43]\n"
      "v_mfma_f32_16x16x32_bf16 v[44:47], v[204:207], v[112:115], v[44:47]\n"
      "v_mfma_f32_16x16x32_bf16 v[48:51], v[208:211], v[112:115], v[48:51]\n"
      "v_mfma_f32_16x16x32_bf16 v[52:55], v[212:215], v[112:115], v[52:55]\n"
      "v_mfma_f32_16x16x32_bf16 v[56:59], v[216:219], v[112:115], v[56:59]\n"
      "v_mfma_f32_16x16x32_bf16 v[60:63], v[220:223], v[112:115], v[60:63]\n"
      "s_cmp_eq_u32 s90, 0\n"
      "s_cbranch_scc1 .Lbdone_%=\n"
      ".Lgd12_%=:\n"
      "global_load_dword v9, v4, %[xq0]\n"
      "s_branch .Lgn12_%=\n"
      ".Lgr12_%=:\n"
      "global_load_dwordx4 v[112:115], v4, s[80:81] offset:0 sc1\n"
      ".Lgn12_%=:\n"
      "s_waitcnt vmcnt(14)\n"
      "s_bitcmp1_b32 s90, 13\n"
      "s_cbranch_scc0 .Lgd13_%=\n"
      "v_max3_u32 v8, v116, v117, v118\n"
      "v_max_u32 v8, v8, v119\n"
      "v_cmp_eq_u32 vcc, -1, v8\n"
      "s_cmp_lt_u32 s96, 4194304\n"
      "s_cselect_b64 vcc, vcc, 0\n"
      "s_cbranch_vccnz .Lgr13_%=\n"
      "s_andn2_b32 s90, s90, 8192\n"
      "v_mfma_f32_16x16x32_bf16 v[32:35], v[224:227], v[116:119], v[32:35]\n"
      "v_mfma_f32_16x16x32_bf16 v[36:39], v[228:231], v[116:119], v[36:39]\n"
      "v_mfma_f32_16x16x32_bf16 v[40:43], v[232:235], v[116:119], v[40:43]\n"
      "v_mfma_f32_16x16x32_bf16 v[44:47], v[236:239], v[116:119], v[44:47]\n"
      "v_mfma_f32_16x16x32_bf16 v[48:51], v[240:243], v[116:119], v[48:51]\n"
      "v_mfma_f32_16x16x32_bf16 v[52:55], v[244:247], v[116:119], v[52:55]\n"
      "v_mfma_f32_16x16x32_bf16 v[56:59], v[248:251], v[116:119], v[56:59]\n"
      "v_mfma_f32_16x16x32_bf16 v[60:63], v[252:255], v[116:119], v[60:63]\n"
      "s_cmp_eq_u32 s90, 0\n"
      "s_cbranch_scc1 .Lbdone_%=\n"
      ".Lgd13_%=:\n"
      "global_load_dword v9, v4, %[xq0]\n"
      "s_branch .Lgn13_%=\n"
      ".Lgr13_%=:\n"
      "global_load_dwordx4 v[116:119], v4, s[80:81] offset:1024 sc1\n"
      ".Lgn13_%=:\n"
      "s_branch .Lround_%=\n"
      ".Lbdone_%=:\n"
      "v_lshlrev_b32 v24, 3, %[ln]\n"
      "v_cvt_pk_bf16_f32 v36, v36, v37\n"
      "v_cvt_pk_bf16_f32 v37, v38, v39\n"
      "v_min_u32 v36, -2, v36\n"
      "v_min_u32 v37, -2, v37\n"
      "global_store_dwordx2 v24, v[36:37], s[40:41] offset:-3584 sc1\n"
      "v_cvt_pk_bf16_f32 v40, v40, v41\n"
      "v_cvt_pk_bf16_f32 v41, v42, v43\n"
      "v_min_u32 v40, -2, v40\n"
      "v_min_u32 v41, -2, v41\n"
      "global_store_dwordx2 v24, v[40:41], s[40:41] offset:-3072 sc1\n"
      "v_cvt_pk_bf16_f32 v44, v44, v45\n"
      "v_cvt_pk_bf16_f32 v45, v46, v47\n"
      "v_min_u32 v44, -2, v44\n"
      "v_min_u32 v45, -2, v45\n"
      "global_store_dwordx2 v24, v[44:45], s[40:41] offset:-2560 sc1\n"
      "v_cvt_pk_bf16_f32 v48, v48, v49\n"
      "v_cvt_pk_bf16_f32 v49, v50, v51\n"
      "v_min_u32 v48, -2, v48\n"
      "v_min_u32 v49, -2, v49\n"
      "global_store_dwordx2 v24, v[48:49], s[40:41] offset:-2048 sc1\n"
      "v_cvt_pk_bf16_f32 v52, v52, v53\n"
      "v_cvt_pk_bf16_f32 v53, v54, v55\n"
      "v_min_u32 v52, -2, v52\n"
      "v_min_u32 v53, -2, v53\n"
      "global_store_dwordx2 v24, v[52:53], s[40:41] offset:-1536 sc1\n"
      "v_cvt_pk_bf16_f32 v56, v56, v57\n"
      "v_cvt_pk_bf16_f32 v57, v58, v59\n"
      "v_min_u32 v56, -2, v56\n"
      "v_min_u32 v57, -2, v57\n"
      "global_store_dwordx2 v24, v[56:57], s[40:41] offset:-1024 sc1\n"
      "v_cvt_pk_bf16_f32 v60, v60, v61\n"
      "v_cvt_pk_bf16_f32 v61, v62, v63\n"
      "v_min_u32 v60, -2, v60\n"
      "v_min_u32 v61, -2, v61\n"
      "global_store_dwordx2 v24, v[60:61], s[40:41] offset:-512 sc1\n"
      "s_nop 0\n"
      ".Lf2_%=:\n"
      "v_lshrrev_b32 v8, 16, v7\n"
      "v_cmp_eq_u32 s[84:85], s94, v6\n"
      "v_cmp_eq_u32 s[86:87], s95, v8\n"
      "s_nop 3\n"
      "s_and_b32 s84, s84, s86\n"
      "s_and_b32 s84, s84, 0xff01\n"
      "s_cmp_eq_u32 s84, 0xff01\n"
      "s_cbranch_scc1 .Lf2ok_%=\n"
      "s_sleep 1\n"
      "global_load_dwordx2 v[6:7], v5, %[ws] sc1\n"
      "s_waitcnt vmcnt(0)\n"
      "s_add_u32 s96, s96, 1\n"
      "s_cmp_lt_u32 s96, 4194304\n"
      "s_cbranch_scc1 .Lf2_%=\n"
      ".Lf2ok_%=:\n"
      "v_mov_b32 v10, 0\n"
      "v_mov_b32 v11, 0\n"
      "s_mov_b64 exec, 0xffff\n"
      "global_store_dwordx2 v5, v[10:11], %[ws] sc1\n"
      "s_mov_b64 exec, -1\n"
      "s_mov_b32 s90, 0xfe\n"
      "global_load_dwordx2 v[132:133], v12, s[46:47] sc1\n"
      "global_load_dwordx2 v[136:137], v13, s[46:47] sc1\n"
      "global_load_dwordx2 v[140:141], v14, s[46:47] sc1\n"
      "global_load_dwordx2 v[144:145], v15, s[46:47] sc1\n"
      "global_load_dwordx2 v[148:149], v16, s[46:47] sc1\n"
      "global_load_dwordx2 v[152:153], v17, s[46:47] sc1\n"
      "global_load_dwordx2 v[156:157], v18, s[46:47] sc1\n"
      ".Lsp_%=:\n"
      "s_add_u32 s96, s96, 1\n"
      "s_waitcnt vmcnt(6)\n"
      "s_bitcmp1_b32 s90, 1\n"
      "s_cbranch_scc0 .Lxd0_1_%=\n"
      "v_max_u32 v10, v132, v133\n"
      "v_cmp_eq_u32 vcc, -1, v10\n"
      "s_cmp_lt_u32 s96, 4194304\n"
      "s_cselect_b64 vcc, vcc, 0\n"
      "s_cbranch_vccnz .Lxr0_1_%=\n"
      "v_lshlrev_b32 v196, 16, v132\n"
      "v_and_b32 v197, 0xffff0000, v132\n"
      "v_lshlrev_b32 v198, 16, v133\n"
      "v_and_b32 v199, 0xffff0000, v133\n"
      "s_bitset0_b32 s90, 1\n"
      "s_cmp_eq_u32 s90, 0\n"
      "s_cbranch_scc1 .Lspok_%=\n"
      ".Lxd0_1_%=:\n"
      "global_load_dword v9, v4, %[xq0]\n"
      "s_branch .Lxn0_1_%=\n"
      ".Lxr0_1_%=:\n"
      "global_load_dwordx2 v[132:133], v12, s[46:47] sc1\n"
      ".Lxn0_1_%=:\n"
      "s_waitcnt vmcnt(6)\n"
      "s_bitcmp1_b32 s90, 2\n"
      "s_cbranch_scc0 .Lxd0_2_%=\n"
      "v_max_u32 v10, v136, v137\n"
      "v_cmp_eq_u32 vcc, -1, v10\n"
      "s_cmp_lt_u32 s96, 4194304\n"
      "s_cselect_b64 vcc, vcc, 0\n"
      "s_cbranch_vccnz .Lxr0_2_%=\n"
      "v_lshlrev_b32 v200, 16, v136\n"
      "v_and_b32 v201, 0xffff0000, v136\n"
      "v_lshlrev_b32 v202, 16, v137\n"
      "v_and_b32 v203, 0xffff0000, v137\n"
      "s_bitset0_b32 s90, 2\n"
      "s_cmp_eq_u32 s90, 0\n"
      "s_cbranch_scc1 .Lspok_%=\n"
      ".Lxd0_2_%=:\n"
      "global_load_dword v9, v4, %[xq0]\n"
      "s_branch .Lxn0_2_%=\n"
      ".Lxr0_2_%=:\n"
      "global_load_dwordx2 v[136:137], v13, s[46:47] sc1\n"
      ".Lxn0_2_%=:\n"
      "s_waitcnt vmcnt(6)\n"
      "s_bitcmp1_b32 s90, 3\n"
      "s_cbranch_scc0 .Lxd0_3_%=\n"
      "v_max_u32 v10, v140, v141\n"
      "v_cmp_eq_u32 vcc, -1, v10\n"
      "s_cmp_lt_u32 s96, 4194304\n"
      "s_cselect_b64 vcc, vcc, 0\n"
      "s_cbranch_vccnz .Lxr0_3_%=\n"
      "v_lshlrev_b32 v204, 16, v140\n"
      "v_and_b32 v205, 0xffff0000, v140\n"
      "v_lshlrev_b32 v206, 16, v141\n"
      "v_and_b32 v207, 0xffff0000, v141\n"
      "s_bitset0_b32 s90, 3\n"
      "s_cmp_eq_u32 s90, 0\n"
      "s_cbranch_scc1 .Lspok_%=\n"
      ".Lxd0_3_%=:\n"
      "global_load_dword v9, v4, %[xq0]\n"
      "s_branch .Lxn0_3_%=\n"
      ".Lxr0_3_%=:\n"
      "global_load_dwordx2 v[140:141], v14, s[46:47] sc1\n"
      ".Lxn0_3_%=:\n"
      "s_waitcnt vmcnt(6)\n"
      "s_bitcmp1_b32 s90, 4\n"
      "s_cbranch_scc0 .Lxd0_4_%=\n"
      "v_max_u32 v10, v144, v145\n"
      "v_cmp_eq_u32 vcc, -1, v10\n"
      "s_cmp_lt_u32 s96, 4194304\n"
      "s_cselect_b64 vcc, vcc, 0\n"
      "s_cbranch_vccnz .Lxr0_4_%=\n"
      "v_lshlrev_b32 v208, 16, v144\n"
      "v_and_b32 v209, 0xffff0000, v144\n"
      "v_lshlrev_b32 v210, 16, v145\n"
      "v_and_b32 v211, 0xffff0000, v145\n"
      "s_bitset0_b32 s90, 4\n"
      "s_cmp_eq_u32 s90, 0\n"
      "s_cbranch_scc1 .Lspok_%=\n"
      ".Lxd0_4_%=:\n"
      "global_load_dword v9, v4, %[xq0]\n"
      "s_branch .Lxn0_4_%=\n"
      ".Lxr0_4_%=:\n"
      "global_load_dwordx2 v[144:145], v15, s[46:47] sc1\n"
      ".Lxn0_4_%=:\n"
      "s_waitcnt vmcnt(6)\n"
      "s_bitcmp1_b32 s90, 5\n"
      "s_cbranch_scc0 .Lxd0_5_%=\n"
      "v_max_u32 v10, v148, v149\n"
      "v_cmp_eq_u32 vcc, -1, v10\n"
      "s_cmp_lt_u32 s96, 4194304\n"
      "s_cselect_b64 vcc, vcc, 0\n"
      "s_cbranch_vccnz .Lxr0_5_%=\n"
      "v_lshlrev_b32 v212, 16, v148\n"
      "v_and_b32 v213, 0xffff0000, v148\n"
      "v_lshlrev_b32 v214, 16, v149\n"
      "v_and_b32 v215, 0xffff0000, v149\n"
      "s_bitset0_b32 s90, 5\n"
      "s_cmp_eq_u32 s90, 0\n"
      "s_cbranch_scc1 .Lspok_%=\n"
      ".Lxd0_5_%=:\n"
      "global_load_dword v9, v4, %[xq0]\n"
      "s_branch .Lxn0_5_%=\n"
      ".Lxr0_5_%=:\n"
      "global_load_dwordx2 v[148:149], v16, s[46:47] sc1\n"
      ".Lxn0_5_%=:\n"
      "s_waitcnt vmcnt(6)\n"
      "s_bitcmp1_b32 s90, 6\n"
      "s_cbranch_scc0 .Lxd0_6_%=\n"
      "v_max_u32 v10, v152, v153\n"
      "v_cmp_eq_u32 vcc, -1, v10\n"
      "s_cmp_lt_u32 s96, 4194304\n"
      "s_cselect_b64 vcc, vcc, 0\n"
      "s_cbranch_vccnz .Lxr0_6_%=\n"
      "v_lshlrev_b32 v216, 16, v152\n"
      "v_and_b32 v217, 0xffff0000, v152\n"
      "v_lshlrev_b32 v218, 16, v153\n"
      "v_and_b32 v219, 0xffff0000, v153\n"
      "s_bitset0_b32 s90, 6\n"
      "s_cmp_eq_u32 s90, 0\n"
      "s_cbranch_scc1 .Lspok_%=\n"
      ".Lxd0_6_%=:\n"
      "global_load_dword v9, v4, %[xq0]\n"
      "s_branch .Lxn0_6_%=\n"
      ".Lxr0_6_%=:\n"
      "global_load_dwordx2 v[152:153], v17, s[46:47] sc1\n"
      ".Lxn0_6_%=:\n"
      "s_waitcnt vmcnt(6)\n"
      "s_bitcmp1_b32 s90, 7\n"
      "s_cbranch_scc0 .Lxd0_7_%=\n"
      "v_max_u32 v10, v156, v157\n"
      "v_cmp_eq_u32 vcc, -1, v10\n"
      "s_cmp_lt_u32 s96, 4194304\n"
      "s_cselect_b64 vcc, vcc, 0\n"
      "s_cbranch_vccnz .Lxr0_7_%=\n"
      "v_lshlrev_b32 v220, 16, v156\n"
      "v_and_b32 v221, 0xffff0000, v156\n"
      "v_lshlrev_b32 v222, 16, v157\n"
      "v_and_b32 v223, 0xffff0000, v157\n"
      "s_bitset0_b32 s90, 7\n"
      "s_cmp_eq_u32 s90, 0\n"
      "s_cbranch_scc1 .Lspok_%=\n"
      ".Lxd0_7_%=:\n"
      "global_load_dword v9, v4, %[xq0]\n"
      "s_branch .Lxn0_7_%=\n"
      ".Lxr0_7_%=:\n"
      "global_load_dwordx2 v[156:157], v18, s[46:47] sc1\n"
      ".Lxn0_7_%=:\n"
      "s_branch .Lsp_%=\n"
      ".Lspok_%=:\n"
      "v_pk_add_f32 v[192:193], v[32:33], v[196:197]\n"
      "v_pk_add_f32 v[194:195], v[34:35], v[198:199]\n"
      "v_pk_add_f32 v[192:193], v[192:193], v[200:201]\n"
      "v_pk_add_f32 v[194:195], v[194:195], v[202:203]\n"
      "v_pk_add_f32 v[192:193], v[192:193], v[204:205]\n"
      "v_pk_add_f32 v[194:195], v[194:195], v[206:207]\n"
      "v_pk_add_f32 v[192:193], v[192:193], v[208:209]\n"
      "v_pk_add_f32 v[194:195], v[194:195], v[210:211]\n"
      "v_pk_add_f32 v[192:193], v[192:193], v[212:213]\n"
      "v_pk_add_f32 v[194:195], v[194:195], v[214:215]\n"
      "v_pk_add_f32 v[192:193], v[192:193], v[216:217]\n"
      "v_pk_add_f32 v[194:195], v[194:195], v[218:219]\n"
      "v_pk_add_f32 v[192:193], v[192:193], v[220:221]\n"
      "v_pk_add_f32 v[194:195], v[194:195], v[222:223]\n"
      "v_cvt_pk_bf16_f32 v196, v192, v193\n"
      "v_cvt_pk_bf16_f32 v197, v194, v195\n"
      "global_store_dwordx2 v19, v[196:197], s[48:49] sc1\n"
      :
      : [voa] "v"(voa), [xo0] "v"(xo0), [wq0] "s"(wq0), [wq1] "s"(wq1), [wq2] "s"(wq2), [wq3] "s"(wq3), [xq0] "s"(xq0), [xq1] "s"(xq1), [xq2] "s"(xq2), [xq3] "s"(xq3), [j] "s"(jj), [lb] "s"(lb), [m0b] "s"(m0b), [w] "s"(w), [ln] "v"(lane), [ws] "s"(ws), [wd] "s"(wd), [out] "s"(out), [xcd] "s"(xcd), [mt4] "s"(mt4), [bb] "s"(b), [ot0] "s"(ot0), [ot1] "s"(ot1)
      : "v4", "v5", "v6", "v7", "v8", "v9", "v10", "v11", "v12", "v13", "v14", "v15", "v16", "v17", "v18", "v19", "v20", "v21", "v22", "v23", "v24", "v25",
        "v26", "v27", "v28", "v29", "v30", "v31", "v32", "v33", "v34", "v35", "v36", "v37", "v38", "v39", "v40", "v41", "v42", "v43", "v44", "v45", "v46",
        "v47", "v48", "v49", "v50", "v51", "v52", "v53", "v54", "v55", "v56", "v57", "v58", "v59", "v60", "v61", "v62", "v63", "v64", "v65", "v66", "v67",
        "v68", "v69", "v70", "v71", "v72", "v73", "v74", "v75", "v76", "v77", "v78", "v79", "v80", "v81", "v82", "v83", "v84", "v85", "v86", "v87", "v88",
        "v89", "v90", "v91", "v92", "v93", "v94", "v95", "v96", "v97", "v98", "v99", "v100", "v101", "v102", "v103", "v104", "v105", "v106", "v107", "v108",
        "v109", "v110", "v111", "v112", "v113", "v114", "v115", "v116", "v117", "v118", "v119", "v120", "v121", "v122", "v123", "v124", "v125", "v126",
        "v127", "v128", "v129", "v130", "v131", "v132", "v133", "v134", "v135", "v136", "v137", "v138", "v139", "v140", "v141", "v142", "v143", "v144",
        "v145", "v146", "v147", "v148", "v149", "v150", "v151", "v152", "v153", "v154", "v155", "v156", "v157", "v158", "v159", "v160", "v161", "v162",
        "v163", "v164", "v165", "v166", "v167", "v168", "v169", "v170", "v171", "v172", "v173", "v174", "v175", "v176", "v177", "v178", "v179", "v180",
        "v181", "v182", "v183", "v184", "v185", "v186", "v187", "v188", "v189", "v190", "v191", "v192", "v193", "v194", "v195", "v196", "v197", "v198",
        "v199", "v200", "v201", "v202", "v203", "v204", "v205", "v206", "v207", "v208", "v209", "v210", "v211", "v212", "v213", "v214", "v215", "v216",
        "v217", "v218", "v219", "v220", "v221", "v222", "v223", "v224", "v225", "v226", "v227", "v228", "v229", "v230", "v231", "v232", "v233", "v234",
        "v235", "v236", "v237", "v238", "v239", "v240", "v241", "v242", "v243", "v244", "v245", "v246", "v247", "v248", "v249", "v250", "v251", "v252",
        "v253", "v254", "v255", "a0", "a1", "a2", "a3", "a4", "a5", "a6", "a7", "a8", "a9", "a10", "a11", "a12", "a13", "a14", "a15", "a16", "a17", "a18",
        "a19", "a20", "a21", "a22", "a23", "a24", "a25", "a26", "a27", "a28", "a29", "a30", "a31", "a32", "a33", "a34", "a35", "a36", "a37", "a38", "a39",
        "a40", "a41", "a42", "a43", "a44", "a45", "a46", "a47", "a48", "a49", "a50", "a51", "a52", "a53", "a54", "a55", "a56", "a57", "a58", "a59", "a60",
        "a61", "a62", "a63", "a64", "a65", "a66", "a67", "a68", "a69", "a70", "a71", "a72", "a73", "a74", "a75", "a76", "a77", "a78", "a79", "a80", "a81",
        "a82", "a83", "a84", "a85", "a86", "a87", "a88", "a89", "a90", "a91", "a92", "a93", "a94", "a95", "a96", "a97", "a98", "a99", "a100", "a101", "a102",
        "a103", "a104", "a105", "a106", "a107", "a108", "a109", "a110", "a111", "a112", "a113", "a114", "a115", "a116", "a117", "a118", "a119", "a120",
        "a121", "a122", "a123", "a124", "a125", "a126", "a127", "a128", "a129", "a130", "a131", "a132", "a133", "a134", "a135", "a136", "a137", "a138",
        "a139", "a140", "a141", "a142", "a143", "a144", "a145", "a146", "a147", "a148", "a149", "a150", "a151", "a152", "a153", "a154", "a155", "a156",
        "a157", "a158", "a159", "a160", "a161", "a162", "a163", "a164", "a165", "a166", "a167", "a168", "a169", "a170", "a171", "a172", "a173", "a174",
        "a175", "a176", "a177", "a178", "a179", "a180", "a181", "a182", "a183", "a184", "a185", "a186", "a187", "a188", "a189", "a190", "a191", "a192",
        "a193", "a194", "a195", "a196", "a197", "a198", "a199", "a200", "a201", "a202", "a203", "a204", "a205", "a206", "a207", "a208", "a209", "a210",
        "a211", "a212", "a213", "a214", "a215", "a216", "a217", "a218", "a219", "a220", "a221", "a222", "a223", "a224", "a225", "a226", "a227", "a228",
        "a229", "a230", "a231", "a232", "a233", "a234", "a235", "a236", "a237", "a238", "a239", "a240", "a241", "a242", "a243", "a244", "a245", "a246",
        "a247", "a248", "a249", "a250", "a251", "a252", "a253", "a254", "a255", "s40", "s41", "s42", "s43", "s44", "s45", "s46", "s47", "s48", "s49", "s50",
        "s51", "s52", "s53", "s54", "s55", "s56", "s57", "s58", "s59", "s60", "s61", "s62", "s63", "s64", "s65", "s66", "s67", "s68", "s69", "s70", "s71",
        "s72", "s73", "s74", "s75", "s76", "s77", "s78", "s79", "s80", "s81", "s82", "s83", "s84", "s85", "s86", "s87", "s88", "s89", "s90", "s91", "s92",
        "s93", "s94", "s95", "s96", "s97", "s98", "s99", "vcc", "scc", "memory"
  );
}

extern "C" void kernel_launch(void* const* d_in, const int* in_sizes, int n_in, void* d_out, int out_size, void* d_ws,
                              size_t ws_size, hipStream_t stream) {
  k_ffn<<<256, 256, 0, stream>>>((const uint16_t*)d_in[0], (const uint16_t*)d_in[1], (const uint16_t*)d_in[2],
                                 (const uint16_t*)d_in[3], (uint16_t*)d_out, (unsigned char*)d_ws);
}
```
